```python
import torch

def _softmax(x, axis=-1):
    return torch.softmax(x, dim=axis)
def _gelu(x):
    return torch.nn.functional.gelu(x, approximate="tanh")
def _silu(x):
    return torch.nn.functional.silu(x)
def _sigmoid(x):
    return torch.sigmoid(x)
def _layernorm(x, axis=-1, eps=1e-5):
    m = x.mean(dim=axis, keepdim=True)
    v = x.var(dim=axis, keepdim=True, unbiased=False)
    return (x - m) / torch.sqrt(v + eps)
def _scatter_add(tgt, idx, src, axis=0):
    return tgt.clone().index_add_(axis, idx, src)
def _segment_sum(src, seg, n):
    out = torch.zeros((n,) + src.shape[1:], dtype=src.dtype)
    return out.index_add_(0, seg, src)
def _pad(x, pw):
    flat = tuple(v for pair in reversed(pw) for v in pair)
    return torch.nn.functional.pad(x, flat)

kernel_name = 'recurrent_scan__sliding_block__glu_gated_bilinear__prompt'

def setup_inputs():

    _w = (lambda a: (lambda t: t.to(torch.bfloat16) if t.is_floating_point() else t)(torch.as_tensor(a)))
    def _A_setup():
        B, S, D = 1, 2048, 448
        return {
            "x": _w((lambda *s: torch.randn(*s, dtype=torch.bfloat16) * 0.05)(B, S, D)),
            "wqkv": _w((lambda *s: torch.randn(*s, dtype=torch.bfloat16) * 0.2)(D, 3 * D)),
            "wo": _w((lambda *s: torch.randn(*s, dtype=torch.bfloat16) * 0.2)(D, D)),
        }
    _A_in = _A_setup()

    def _B_setup():
        B, S, D, F = 1, 2048, 448, 1792
        return {
            "x": _w((lambda *s: torch.randn(*s, dtype=torch.bfloat16) * 0.05)(B, S, D)),
            "w_gate": _w((lambda *s: torch.randn(*s, dtype=torch.bfloat16) * 0.1)(D, F) * (D ** -0.5)),
            "w_up": _w((lambda *s: torch.randn(*s, dtype=torch.bfloat16) * 0.1)(D, F) * (D ** -0.5)),
            "w_down": _w((lambda *s: torch.randn(*s, dtype=torch.bfloat16) * 0.1)(F, D) * (F ** -0.5)),
        }
    _B_in = _B_setup()

    return {
        "A_x": _A_in["x"],
        "A_wqkv": _A_in["wqkv"],
        "A_wo": _A_in["wo"],
        "B_x": _B_in["x"],
        "B_w_gate": _B_in["w_gate"],
        "B_w_up": _B_in["w_up"],
        "B_w_down": _B_in["w_down"],
    }

def reference(A_x, A_wqkv, A_wo, B_x, B_w_gate, B_w_up, B_w_down):
    def _A_ref(x, wqkv, wo):
        B, S, D = x.shape
        H = 16; Dh = D // H; W = 256
        qkv = (lambda x, *d: x.permute(*d))((x @ wqkv).reshape(B, S, 3, H, Dh), 2, 0, 3, 1, 4)
        q, k, v = qkv[0], qkv[1], qkv[2]
        scores = torch.einsum("bhqd,bhkd->bhqk", q, k) * (Dh ** -0.5)
        idx = torch.arange(S)
        mask = (idx[:, None] - idx[None, :] >= 0) & (idx[:, None] - idx[None, :] < W)
        scores = torch.where(mask[None, None], scores, -1e9)
        p = _softmax(scores, axis=-1)
        o = torch.einsum("bhqk,bhkd->bhqd", p, v)
        return (lambda x, *d: x.permute(*d))(o, 0, 2, 1, 3).reshape(B, S, D) @ wo

    def _B_ref(x, w_gate, w_up, w_down):
        g = torch.einsum("bsd,df->bsf", x, w_gate)
        u = torch.einsum("bsd,df->bsf", x, w_up)
        h = g * u
        return torch.einsum("bsf,fd->bsd", h, w_down)

    x = A_x
    xn = _layernorm(x, axis=-1)
    s = _A_ref(xn, A_wqkv, A_wo)
    g = _silu(_B_ref(xn, B_w_gate, B_w_up, B_w_down))
    return x + s * g
```

```cpp
#include <hip/hip_runtime.h>
#include <hip/hip_bf16.h>
#include <stdint.h>

#define LAS __attribute__((address_space(3)))
#define GAS __attribute__((address_space(1)))
#define DI __device__ __forceinline__

typedef unsigned short u16;
typedef __bf16 bf16x8 __attribute__((ext_vector_type(8)));
typedef __bf16 bf16x2 __attribute__((ext_vector_type(2)));
typedef short s16x4 __attribute__((ext_vector_type(4)));
typedef short s16x8 __attribute__((ext_vector_type(8)));
typedef float f32x4 __attribute__((ext_vector_type(4)));
typedef float f32x16 __attribute__((ext_vector_type(16)));
typedef unsigned u32x4 __attribute__((ext_vector_type(4)));
typedef unsigned u32x2 __attribute__((ext_vector_type(2)));

constexpr int S_ = 2048, D_ = 448, F_ = 1792, NQKV = 1344;
constexpr size_t WS_QF = 0, WS_KF = 2u << 20, WS_VF = 4u << 20, WS_OF = 6u << 20, WS_HF = 8u << 20;

DI void glds16(const void* g, LAS char* l) { __builtin_amdgcn_global_load_lds((const GAS void*)g, (LAS void*)l, 16, 0, 0); }
DI bf16x8 tr_frag(LAS char* p, int off0, int off1) {
  s16x4 lo = __builtin_amdgcn_ds_read_tr16_b64_v4i16((LAS s16x4*)(p + off0));
  s16x4 hi = __builtin_amdgcn_ds_read_tr16_b64_v4i16((LAS s16x4*)(p + off1));
  s16x8 v = __builtin_shufflevector(lo, hi, 0, 1, 2, 3, 4, 5, 6, 7);
  return __builtin_bit_cast(bf16x8, v);
}
DI unsigned pk2(float a, float b) { bf16x2 c; c[0] = (__bf16)a; c[1] = (__bf16)b; return __builtin_bit_cast(unsigned, c); }
DI float bflo(unsigned u) { return __uint_as_float(u << 16); }
DI float bfhi(unsigned u) { return __uint_as_float(u & 0xffff0000u); }
#define MFMA32(a, b, c) __builtin_amdgcn_mfma_f32_32x32x16_bf16((a), (b), (c), 0, 0, 0)
#define MFMA16(a, b, c) __builtin_amdgcn_mfma_f32_16x16x32_bf16((a), (b), (c), 0, 0, 0)

__global__ __launch_bounds__(512, 2) void kA(const u16* __restrict__ x, const u16* __restrict__ wqkv, const u16* __restrict__ wg,
                                            const u16* __restrict__ wu, char* __restrict__ ws) {
  extern __shared__ __attribute__((aligned(16))) char smem_[];
  LAS char* smem = (LAS char*)smem_;
  const int tid = threadIdx.x, lane = tid & 63, w = __builtin_amdgcn_readfirstlane(tid >> 6);
  const int bid = blockIdx.x, xcd = bid & 7, ii = bid >> 3;
  const int r = (xcd & 1) * 8 + (ii & 7), c = (xcd >> 1) * 4 + (ii >> 3);
  const int tb = w & 3, ch = w >> 2;
  const int m = lane & 31, h = lane >> 5;
  for (int j = 0; j < 14; ++j) {
    const int idx = w * 14 + j, tbi = idx / 28, rem = idx % 28, cb = rem >> 2, rg = rem & 3;
    const int mm = rg * 8 + (lane >> 3), cc = (lane & 7) ^ ((mm >> 1) & 7);
    glds16(x + (size_t)(r * 128 + tbi * 32 + mm) * D_ + cb * 64 + cc * 8, smem + idx * 1024);
  }
  __syncthreads();
  u32x4 xr[28];
  {
    const int f = (m >> 1) & 7;
    LAS char* xb = smem + tb * 28672 + (m >> 3) * 1024 + (m & 7) * 128;
#pragma unroll
    for (int s = 0; s < 28; ++s) xr[s] = *(LAS u32x4*)(xb + (s >> 2) * 4096 + (((2 * (s & 3) + h) ^ f) * 16));
#pragma unroll
    for (int s = 0; s < 28; ++s) asm volatile("" : "+v"(xr[s]));
  }
  __syncthreads();
  const int scc = lane >> 5, srr = (lane >> 2) & 7, sp = lane & 3;
  auto stage_unit = [&](int buf, const u16* W, int ld, int c0, int c1) {
    const u16* g0 = W + (size_t)srr * ld + (scc ? c1 : c0) + 8 * sp;
#pragma unroll
    for (int j = 0; j < 7; ++j) { const int i = w * 7 + j; glds16(g0 + (size_t)(8 * i) * ld, smem + buf * 57344 + i * 1024); }
  };
  const int nq = c < 12 ? 1 : 3;
  const int qb0 = c < 12 ? c : 12 + 3 * (c - 12);
  const int gb0 = c < 12 ? 2 * c : 24 + (c - 12);
  auto stage_idx = [&](int u) {
    const int buf = u & 1;
    if (u < nq) { const int b = qb0 + u; const int c0 = 56 * b; int c1 = 56 * b + 28; if (c1 > 1312) c1 = 1312; stage_unit(buf, wqkv, NQKV, c0, c1); }
    else { const int v = u - nq; const int gb = gb0 + (v >> 1); stage_unit(buf, (v & 1) ? wu : wg, F_, 64 * gb, 64 * gb + 32); }
  };
  stage_idx(0);
  {
    float sum = 0.f;
#pragma unroll
    for (int s = 0; s < 28; ++s)
#pragma unroll
      for (int e = 0; e < 4; ++e) sum += bflo(xr[s][e]) + bfhi(xr[s][e]);
    sum += __shfl_xor(sum, 32);
#pragma unroll
    for (int s = 0; s < 28; ++s) asm volatile("" : "+v"(xr[s]));
    const float mean = sum * (1.f / 448.f);
    float sq = 0.f;
#pragma unroll
    for (int s = 0; s < 28; ++s)
#pragma unroll
      for (int e = 0; e < 4; ++e) { float a = bflo(xr[s][e]) - mean, b = bfhi(xr[s][e]) - mean; sq += a * a + b * b; }
    sq += __shfl_xor(sq, 32);
#pragma unroll
    for (int s = 0; s < 28; ++s) asm volatile("" : "+v"(xr[s]));
    const float rstd = 1.f / sqrtf(sq * (1.f / 448.f) + 1e-5f);
#pragma unroll
    for (int s = 0; s < 28; ++s) {
      u32x4 o;
#pragma unroll
      for (int e = 0; e < 4; ++e) o[e] = pk2((bflo(xr[s][e]) - mean) * rstd, (bfhi(xr[s][e]) - mean) * rstd);
      xr[s] = o;
    }
  }
  __builtin_amdgcn_sched_barrier(0);
#define XF(s) __builtin_bit_cast(bf16x8, xr[s])
  const int blk = (lane >> 4) & 1, q = (lane & 15) >> 2, p = lane & 3;
  const int woff = h * 1024 + ch * 512 + q * 64 + blk * 32 + p * 8;
  const int tbg = r * 4 + tb;
  f32x16 accg;
  for (int u = 0; u < 5; ++u) {
    __syncthreads();
    if (u + 1 < 5) stage_idx(u + 1);
    LAS char* wb = smem + (u & 1) * 57344 + woff;
    f32x16 acc;
#pragma unroll
    for (int i = 0; i < 16; ++i) acc[i] = 0.f;
    const bool isq = u < nq;
    const int t = 2 * (qb0 + u) + ch;
    if (isq && t >= 32) {
#pragma unroll
      for (int s = 0; s < 28; ++s) { acc = MFMA32(XF(s), tr_frag(wb, s * 2048, s * 2048 + 256), acc); if ((s & 3) == 3) __builtin_amdgcn_sched_barrier(0); }
      const int head = t - 32, shift = (t == 47) ? 4 : 0;
      const int d = (m - shift) & 31;
      if (d >= 28) {
#pragma unroll
        for (int i = 0; i < 16; ++i) acc[i] = (d == 28) ? 1.f : 0.f;
      }
      u32x4 p0 = {pk2(acc[0], acc[1]), pk2(acc[2], acc[3]), pk2(acc[8], acc[9]), pk2(acc[10], acc[11])};
      u32x4 p1 = {pk2(acc[4], acc[5]), pk2(acc[6], acc[7]), pk2(acc[12], acc[13]), pk2(acc[14], acc[15])};
      char* base = ws + WS_VF + ((size_t)((head * 64 + tbg) * 2 + (d >> 4)) * 64 + (d & 15)) * 16;
      *(u32x4*)(base + (h) * 256) = p0;
      *(u32x4*)(base + (2 + h) * 256) = p1;
    } else {
#pragma unroll
      for (int s = 0; s < 28; ++s) { acc = MFMA32(tr_frag(wb, s * 2048, s * 2048 + 256), XF(s), acc); if ((s & 3) == 3) __builtin_amdgcn_sched_barrier(0); }
      if (isq) {
        const int head = t & 15;
        if (h == 1) { acc[12] = 0.f; acc[13] = 0.f; acc[14] = 0.f; acc[15] = 0.f; }
        u32x4 p0 = {pk2(acc[0], acc[1]), pk2(acc[2], acc[3]), pk2(acc[4], acc[5]), pk2(acc[6], acc[7])};
        u32x4 p1 = {pk2(acc[8], acc[9]), pk2(acc[10], acc[11]), pk2(acc[12], acc[13]), pk2(acc[14], acc[15])};
        char* base = ws + (t < 16 ? WS_QF : WS_KF) + ((size_t)(head * 128 + tbg * 2 + (m >> 4)) * 64 + (m & 15)) * 16;
        *(u32x4*)(base + (h) * 256) = p0;
        *(u32x4*)(base + (2 + h) * 256) = p1;
      } else {
        const int v = u - nq;
        if ((v & 1) == 0) accg = acc;
        else {
          const int ft = 2 * (gb0 + (v >> 1)) + ch;
          u32x4 p0, p1;
#pragma unroll
          for (int e = 0; e < 4; ++e) { p0[e] = pk2(acc[2 * e] * accg[2 * e], acc[2 * e + 1] * accg[2 * e + 1]);
                                        p1[e] = pk2(acc[8 + 2 * e] * accg[8 + 2 * e], acc[9 + 2 * e] * accg[9 + 2 * e]); }
          char* base = ws + WS_HF + ((size_t)(tbg * 112 + ft * 2) * 64 + lane) * 16;
          *(u32x4*)(base) = p0;
          *(u32x4*)(base + 1024) = p1;
        }
      }
    }
  }
}

__global__ __launch_bounds__(512, 2) void kB(char* __restrict__ ws) {
  extern __shared__ __attribute__((aligned(16))) char smem_[];
  LAS char* smem = (LAS char*)smem_;
  const int tid = threadIdx.x, lane = tid & 63, w = __builtin_amdgcn_readfirstlane(tid >> 6);
  const int bid = blockIdx.x, xcd = bid & 7, ii = bid >> 3;
  const int head = xcd + 8 * (ii >> 4), qb = ii & 15;
  const int kt0 = qb * 8 - 16;
  for (int j = 0; j < 6; ++j) {
    const int i = w * 6 + j;
    if (i < 24) { int kt = kt0 + i; if (kt < 0) kt = 0; glds16(ws + WS_KF + ((size_t)(head * 128 + kt) * 64 + lane) * 16, smem + i * 1024); }
    else { const int jj = i - 24; int kb = (kt0 >> 1) + (jj >> 1); if (kb < 0) kb = 0;
           glds16(ws + WS_VF + ((size_t)((head * 64 + kb) * 2 + (jj & 1)) * 64 + lane) * 16, smem + i * 1024); }
  }
  const bf16x8 qf = *(const bf16x8*)(ws + WS_QF + ((size_t)(head * 128 + qb * 8 + w) * 64 + lane) * 16);
  __syncthreads();
  const int mq = lane & 15, g = lane >> 4;
  const int t0 = w & ~1; const bool even = (w & 1) == 0;
  const float NEG = -__builtin_inff();
  f32x4 st[18];
#pragma unroll
  for (int T = 0; T < 18; ++T) {
    f32x4 cin;
    const bool neg = (kt0 + t0 + T) < 0;
#pragma unroll
    for (int e = 0; e < 4; ++e) {
      const int row = 4 * g + e;
      float v = 0.f;
      const float edge = row > mq ? 0.f : NEG, diag = row <= mq ? 0.f : NEG;
      if (T == 0) v = even ? edge : NEG;
      if (T == 1) v = even ? 0.f : edge;
      if (T == 16) v = even ? diag : 0.f;
      if (T == 17) v = even ? NEG : diag;
      cin[e] = neg ? NEG : v;
    }
    const bf16x8 kf = *(LAS bf16x8*)(smem + (t0 + T) * 1024 + lane * 16);
    st[T] = MFMA16(kf, qf, cin);
  }
  float mx = NEG;
#pragma unroll
  for (int T = 0; T < 18; ++T)
#pragma unroll
    for (int e = 0; e < 4; ++e) mx = fmaxf(mx, st[T][e]);
  mx = fmaxf(mx, __shfl_xor(mx, 16));
  mx = fmaxf(mx, __shfl_xor(mx, 32));
  const float C2 = 0.18898223650461362f * 1.4426950408889634f;
  const float mc = -mx * C2;
  f32x4 o0 = {0.f, 0.f, 0.f, 0.f}, o1 = {0.f, 0.f, 0.f, 0.f};
#pragma unroll
  for (int a = 0; a < 9; ++a) {
    u32x4 pp;
    float e0[4], e1[4];
#pragma unroll
    for (int e = 0; e < 4; ++e) { e0[e] = __builtin_amdgcn_exp2f(fmaf(st[2 * a][e], C2, mc)); e1[e] = __builtin_amdgcn_exp2f(fmaf(st[2 * a + 1][e], C2, mc)); }
    pp[0] = pk2(e0[0], e0[1]); pp[1] = pk2(e0[2], e0[3]); pp[2] = pk2(e1[0], e1[1]); pp[3] = pk2(e1[2], e1[3]);
    const bf16x8 pf = __builtin_bit_cast(bf16x8, pp);
    LAS char* vb = smem + 24576 + ((t0 >> 1) + a) * 2048 + lane * 16;
    o0 = MFMA16(*(LAS bf16x8*)(vb), pf, o0);
    o1 = MFMA16(*(LAS bf16x8*)(vb + 1024), pf, o1);
  }
  const float l = __shfl(o1[0], 48 + mq);
  const float rl = 1.f / l;
  u32x4 ov;
  ov[0] = pk2(o0[0] * rl, o0[1] * rl); ov[1] = pk2(o0[2] * rl, o0[3] * rl);
  if (g == 3) { ov[2] = 0; ov[3] = 0; } else { ov[2] = pk2(o1[0] * rl, o1[1] * rl); ov[3] = pk2(o1[2] * rl, o1[3] * rl); }
  const int tok = qb * 128 + w * 16 + mq;
  char* dst = ws + WS_OF + ((size_t)(((tok >> 5) * 16 + head) * 2 + (g >> 1)) * 64 + (tok & 31) + 32 * (g & 1)) * 16;
  *(u32x4*)dst = ov;
}

__global__ __launch_bounds__(512, 2) void kC(const u16* __restrict__ x, const u16* __restrict__ wo, const u16* __restrict__ wd,
                                            const char* __restrict__ ws, u16* __restrict__ out) {
  extern __shared__ __attribute__((aligned(16))) char smem_[];
  LAS char* smem = (LAS char*)smem_;
  const int tid = threadIdx.x, lane = tid & 63, w = __builtin_amdgcn_readfirstlane(tid >> 6);
  const int bid = blockIdx.x;
  const int xcd = bid & 7, ii = bid >> 3;
  const int tt64 = xcd * 4 + (ii & 3), ct64 = ii >> 2;
  const int tt = w & 1, ct = (w >> 1) & 1, kh = w >> 2;
  const int h = lane >> 5, m = lane & 31;
  const int blk = (lane >> 4) & 1, q = (lane & 15) >> 2, p = lane & 3;
  const int scc = lane >> 5, srr = (lane >> 2) & 7, sp = lane & 3;
  const int tbg = tt64 * 2 + tt;
  const int n0 = ct64 * 64;
  f32x16 accy, accs;
#pragma unroll
  for (int i = 0; i < 16; ++i) { accy[i] = 0.f; accs[i] = 0.f; }
  for (int rd = 0; rd < 2; ++rd) {
    __syncthreads();
    for (int j = 0; j < 14; ++j) {
      const int i = w * 14 + j;
      const int hf = i / 56, b8 = i % 56;
      const int row = hf * 896 + rd * 448 + b8 * 8 + srr;
      glds16(wd + (size_t)row * D_ + n0 + scc * 32 + sp * 8, smem + i * 1024);
    }
    __syncthreads();
    LAS char* wb = smem + kh * 57344 + ct * 512 + blk * 32 + p * 8 + q * 64 + h * 256;
    const char* hb = ws + WS_HF + ((size_t)(tbg * 112 + kh * 56 + rd * 28) * 64 + lane) * 16;
#pragma unroll 4
    for (int s = 0; s < 28; ++s) {
      const bf16x8 bfr = *(const bf16x8*)(hb + (size_t)s * 1024);
      accy = MFMA32(tr_frag(wb, s * 2048, s * 2048 + 1024), bfr, accy);
    }
  }
  __syncthreads();
  for (int j = 0; j < 8; ++j) {
    const int i = w * 8 + j;
    const int hf = i >> 5, b8 = i & 31;
    if (b8 < 29) { int row = hf * 224 + b8 * 8 + srr; if (row > 447) row = 447;
      glds16(wo + (size_t)row * D_ + n0 + scc * 32 + sp * 8, smem + i * 1024); }
  }
  __syncthreads();
  {
    const char* ob = ws + WS_OF + ((size_t)((tbg * 16 + kh * 8) * 2) * 64 + lane) * 16;
#pragma unroll
    for (int hd = 0; hd < 8; ++hd)
#pragma unroll
      for (int s = 0; s < 2; ++s) {
        const int r0 = 28 * hd + 8 * s + 4 * h + q, r1 = r0 + 16;
        LAS char* base = smem + kh * 32768 + ct * 512 + blk * 32 + p * 8;
        s16x4 lo = __builtin_amdgcn_ds_read_tr16_b64_v4i16((LAS s16x4*)(base + (r0 >> 3) * 1024 + (r0 & 7) * 64));
        s16x4 hi = __builtin_amdgcn_ds_read_tr16_b64_v4i16((LAS s16x4*)(base + (r1 >> 3) * 1024 + (r1 & 7) * 64));
        s16x8 v = __builtin_shufflevector(lo, hi, 0, 1, 2, 3, 4, 5, 6, 7);
        const bf16x8 bfr = *(const bf16x8*)(ob + (size_t)(hd * 2 + s) * 1024);
        accs = MFMA32(__builtin_bit_cast(bf16x8, v), bfr, accs);
      }
  }
  __syncthreads();
  LAS f32x4* red = (LAS f32x4*)(smem + (w & 3) * 8192);
  if (kh == 1) {
#pragma unroll
    for (int i = 0; i < 4; ++i) { red[i * 64 + lane] = (f32x4){accy[4 * i], accy[4 * i + 1], accy[4 * i + 2], accy[4 * i + 3]};
                                  red[(4 + i) * 64 + lane] = (f32x4){accs[4 * i], accs[4 * i + 1], accs[4 * i + 2], accs[4 * i + 3]}; }
  }
  __syncthreads();
  if (kh == 0) {
    const size_t rowoff = (size_t)(tbg * 32 + m) * D_ + n0 + ct * 32 + 4 * h;
#pragma unroll
    for (int i = 0; i < 4; ++i) {
      const f32x4 y2 = red[i * 64 + lane], s2 = red[(4 + i) * 64 + lane];
      const u32x2 xv = *(const u32x2*)(x + rowoff + 8 * i);
      float o[4];
#pragma unroll
      for (int e = 0; e < 4; ++e) {
        const float y = accy[4 * i + e] + y2[e], s = accs[4 * i + e] + s2[e];
        const float sg = y / (1.f + __expf(-y));
        const float xe = (e & 1) ? bfhi(xv[e >> 1]) : bflo(xv[e >> 1]);
        o[e] = xe + s * sg;
      }
      u32x2 ov = {pk2(o[0], o[1]), pk2(o[2], o[3])};
      *(u32x2*)(out + rowoff + 8 * i) = ov;
    }
  }
}

extern "C" void kernel_launch(void* const* d_in, const int* in_sizes, int n_in, void* d_out, int out_size, void* d_ws, size_t ws_size,
                              hipStream_t stream) {
  const u16* x = (const u16*)d_in[0];
  const u16* wqkv = (const u16*)d_in[1];
  const u16* wo = (const u16*)d_in[2];
  const u16* wg = (const u16*)d_in[4];
  const u16* wu = (const u16*)d_in[5];
  const u16* wd = (const u16*)d_in[6];
  char* ws = (char*)d_ws;
  hipFuncSetAttribute((const void*)kA, hipFuncAttributeMaxDynamicSharedMemorySize, 114688);
  hipFuncSetAttribute((const void*)kB, hipFuncAttributeMaxDynamicSharedMemorySize, 49152);
  hipFuncSetAttribute((const void*)kC, hipFuncAttributeMaxDynamicSharedMemorySize, 114688);
  kA<<<256, 512, 114688, stream>>>(x, wqkv, wg, wu, ws);
  kB<<<256, 512, 49152, stream>>>(ws);
  kC<<<224, 512, 114688, stream>>>(x, wo, wd, ws, (u16*)d_out);
}
```

```cpp
#include <hip/hip_runtime.h>
#include <hip/hip_bf16.h>
#include <stdint.h>

#define LAS __attribute__((address_space(3)))
#define GAS __attribute__((address_space(1)))
#define DI __device__ __forceinline__

typedef unsigned short u16;
typedef unsigned long long u64;
typedef __bf16 bf16x8 __attribute__((ext_vector_type(8)));
typedef __bf16 bf16x2 __attribute__((ext_vector_type(2)));
typedef short s16x4 __attribute__((ext_vector_type(4)));
typedef short s16x8 __attribute__((ext_vector_type(8)));
typedef float f32x4 __attribute__((ext_vector_type(4)));
typedef float f32x16 __attribute__((ext_vector_type(16)));
typedef unsigned u32x4 __attribute__((ext_vector_type(4)));
typedef unsigned u32x2 __attribute__((ext_vector_type(2)));
typedef __amdgpu_buffer_rsrc_t rsrc_t;

constexpr int S_ = 2048, D_ = 448, F_ = 1792, NQKV = 1344;
constexpr unsigned WS_QF = 0, WS_KF = 2u << 20, WS_VF = 4u << 20, WS_OF = 6u << 20, WS_HF = 8u << 20, WS_XF = 16u << 20, WS_FL = 18u << 20, WS_BYTES = 19u << 20;
constexpr unsigned FL_LN = 0, FL_Q = 512, FL_H = 2560, FL_O = 4608;
constexpr unsigned SPIN_CAP = 1u << 16;

#define LNSLEEP_A 8
#define LNSLEEP_B 70
#define SBAR() __builtin_amdgcn_sched_barrier(0)
#define PIN(x) asm volatile("" : "+v"(x))
#define WAIT_VM(N) asm volatile("s_waitcnt vmcnt(" #N ")" ::: "memory")
#define BAR() asm volatile("s_barrier" ::: "memory")
#define WAIT_BAR(N) asm volatile("s_waitcnt vmcnt(" #N ")\n\ts_barrier" ::: "memory")
#define WAIT_BAR_L(N) asm volatile("s_waitcnt vmcnt(" #N ") lgkmcnt(0)\n\ts_barrier" ::: "memory")
#define LBAR() asm volatile("s_waitcnt lgkmcnt(0)\n\ts_barrier" ::: "memory")

DI rsrc_t mk(const void* p, unsigned bytes) { return __builtin_amdgcn_make_buffer_rsrc((void*)p, (short)0, (int)bytes, 0x00020000); }
DI void bdma(unsigned ldsaddr, unsigned voff, rsrc_t rs, unsigned soff) {
  asm volatile("s_mov_b32 m0, %0\n\ts_nop 0\n\tbuffer_load_dwordx4 %1, %2, %3 offen lds" :: "s"(ldsaddr), "v"(voff), "s"(rs), "s"(soff) : "memory");
}
DI void bdma_sc1(unsigned ldsaddr, unsigned voff, rsrc_t rs, unsigned soff) {
  asm volatile("s_mov_b32 m0, %0\n\ts_nop 0\n\tbuffer_load_dwordx4 %1, %2, %3 offen sc1 lds" :: "s"(ldsaddr), "v"(voff), "s"(rs), "s"(soff) : "memory");
}
DI bf16x8 tr_frag(LAS char* p, int off0, int off1) {
  s16x4 lo = __builtin_amdgcn_ds_read_tr16_b64_v4i16((LAS s16x4*)(p + off0));
  s16x4 hi = __builtin_amdgcn_ds_read_tr16_b64_v4i16((LAS s16x4*)(p + off1));
  s16x8 v = __builtin_shufflevector(lo, hi, 0, 1, 2, 3, 4, 5, 6, 7);
  return __builtin_bit_cast(bf16x8, v);
}
DI unsigned pk2(float a, float b) { bf16x2 c; c[0] = (__bf16)a; c[1] = (__bf16)b; return __builtin_bit_cast(unsigned, c); }
DI float bflo(unsigned u) { return __uint_as_float(u << 16); }
DI float bfhi(unsigned u) { return __uint_as_float(u & 0xffff0000u); }
DI float bfr(float a) { return bflo(pk2(a, 0.f)); }
#define MFMA32(a, b, c) __builtin_amdgcn_mfma_f32_32x32x16_bf16((a), (b), (c), 0, 0, 0)
#define MFMA16(a, b, c) __builtin_amdgcn_mfma_f32_16x16x32_bf16((a), (b), (c), 0, 0, 0)
#define ST128(v, rs, voff, soff) __builtin_amdgcn_raw_buffer_store_b128((v), (rs), (voff) + (soff), 0, 16)
#define LD128(rs, voff, soff) __builtin_amdgcn_raw_buffer_load_b128((rs), (voff), (soff), 16)
#define BC8(v) __builtin_bit_cast(bf16x8, v)

#define STAMPW(i, WV)
#define STAMP(i) STAMPW(i, 0)

template <int CTRL> DI float dpp_add(float v) {
  return v + __builtin_bit_cast(float, __builtin_amdgcn_update_dpp(0, __builtin_bit_cast(int, v), CTRL, 0xf, 0xf, true));
}
DI float wave_sum(float v) {
  v = dpp_add<0x111>(v); v = dpp_add<0x112>(v); v = dpp_add<0x114>(v); v = dpp_add<0x118>(v);
  const float r0 = __builtin_bit_cast(float, __builtin_amdgcn_readlane(__builtin_bit_cast(int, v), 15));
  const float r1 = __builtin_bit_cast(float, __builtin_amdgcn_readlane(__builtin_bit_cast(int, v), 31));
  const float r2 = __builtin_bit_cast(float, __builtin_amdgcn_readlane(__builtin_bit_cast(int, v), 47));
  const float r3 = __builtin_bit_cast(float, __builtin_amdgcn_readlane(__builtin_bit_cast(int, v), 63));
  return (r0 + r1) + (r2 + r3);
}
DI float row_sum(float v) {
  v = dpp_add<0x124>(v); v = dpp_add<0x128>(v);
  { auto rr = __builtin_amdgcn_permlane16_swap(__float_as_uint(v), __float_as_uint(v), false, false); v = __uint_as_float(rr[0]) + __uint_as_float(rr[1]); }
  { auto rr = __builtin_amdgcn_permlane32_swap(__float_as_uint(v), __float_as_uint(v), false, false); v = __uint_as_float(rr[0]) + __uint_as_float(rr[1]); }
  return v;
}
DI GAS u64* flag_ptr(char* ws, int idx) { return (GAS u64*)(ws + WS_FL + 256 + (size_t)idx * 8); }
DI u64 flag_ld(char* ws, int idx) { return __hip_atomic_load(flag_ptr(ws, idx < 0 ? 0 : idx), __ATOMIC_RELAXED, __HIP_MEMORY_SCOPE_AGENT); }
DI void poll_flags(char* ws, int idx, u64 epoch) {
  for (unsigned spins = 0; spins < SPIN_CAP; ++spins) {
    const u64 v = flag_ld(ws, idx);
    if (__all(idx < 0 || v == epoch)) break;
    __builtin_amdgcn_s_sleep(3);
  }
  __builtin_amdgcn_fence(__ATOMIC_ACQUIRE, "wavefront");
}
DI void set_flag(char* ws, int idx, u64 epoch) { __hip_atomic_store(flag_ptr(ws, idx), epoch, __ATOMIC_RELAXED, __HIP_MEMORY_SCOPE_AGENT); }

__global__ __launch_bounds__(512, 2) void mega(const u16* __restrict__ x, const u16* __restrict__ wqkv, const u16* __restrict__ wo,
                                              const u16* __restrict__ wg, const u16* __restrict__ wu, const u16* __restrict__ wd,
                                              u16* __restrict__ out, char* __restrict__ ws) {
  extern __shared__ __attribute__((aligned(16))) char smem_[];
  LAS char* smem = (LAS char*)smem_;
  const unsigned lds0 = (unsigned)(uintptr_t)smem;
  const int tid = threadIdx.x, lane = tid & 63, w = __builtin_amdgcn_readfirstlane(tid >> 6);
  const int bid = blockIdx.x, xcd = bid & 7, ii = bid >> 3;
  const int m = lane & 31, h = lane >> 5;
  const int blk = (lane >> 4) & 1, q4 = (lane & 15) >> 2, p4 = lane & 3;
  const rsrc_t rs_ws = mk(ws, WS_BYTES);
  const unsigned l16 = (unsigned)lane * 16;
  STAMP(0);
  const int r = (xcd & 1) * 4 + (ii & 3), c = (xcd >> 1) * 8 + (ii >> 2);
  const bool isP = c >= 16;
  const int pi = c - 16;
  const int tA = isP ? (pi < 8 ? 32 + 2 * pi : 2 * (pi - 8)) : 16 + c;
  const int rot = 7 * ((ii + (ii >> 2)) & 3);
  const bool isLN = xcd < 4;
  const int lne = ii * 4 + xcd;
  const int lpart = lane >> 2, lrow = lane & 3;
  u32x4 xr[4];
#pragma unroll
  for (int i = 0; i < 4; ++i) xr[i] = (u32x4){0, 0, 0, 0};
  if (isLN && w >= 4) {
    const u16* xp = x + (size_t)(lne * 16 + (w - 4) * 4 + lrow) * D_ + lpart * 8;
#pragma unroll
    for (int i = 0; i < 4; ++i) if (i < 3 || lpart < 8) xr[i] = *(const u32x4*)(xp + i * 128);
  }
  const u64 epoch = __hip_atomic_load((GAS u64*)(ws + WS_FL), __ATOMIC_RELAXED, __HIP_MEMORY_SCOPE_AGENT) + 1;
  const int r8 = lane >> 3, k8 = lane & 7, sw = ((r8 >> 1) & 1) << 2;
  const unsigned scr = isP ? 144u * 1024 : 28u * 1024;
  auto stream = [&](int kind) {
    const int hf = kind & 1;
    const bool isq = kind < 2;
    const u16* base = isq ? wqkv : (kind < 4 ? wg : wu);
    const rsrc_t rs = mk(base, isq ? D_ * NQKV * 2 : D_ * F_ * 2);
    const unsigned ld2 = isq ? NQKV * 2 : F_ * 2;
    unsigned vo, lds, lstep; bool act = true;
    if (!isP) {
      if (isq) { vo = (unsigned)((lane >> 2) * (NQKV * 2) + 56 * tA + (lane & 3) * 16); lds = 0; lstep = 1024; }
      else { vo = (unsigned)(r8 * (F_ * 2) + 128 * c + (k8 ^ sw ^ (kind < 4 ? 0 : 2)) * 16); lds = (unsigned)((kind < 4 ? 48 : 104) + hf) * 1024; lstep = 2048; }
    } else {
      if (isq) { int cB = 56 * (tA + 1); if (cB > 2624) cB = 2624; const int ch8 = k8 ^ sw;
                 vo = (unsigned)(r8 * (NQKV * 2) + (ch8 < 4 ? 56 * tA + 16 * ch8 : cB + 16 * (ch8 - 4))); lds = (unsigned)hf * 1024; lstep = 2048; }
      else { const int r6 = lane / 6, k6 = lane - 6 * r6; vo = (unsigned)(r6 * (F_ * 2) + 2 * (1024 + 48 * pi) + 16 * k6);
             lds = (kind < 4 ? 57344u : 100480u) + (unsigned)hf * 768; lstep = 1536; act = lane < 48; }
    }
    const unsigned so0 = (!isP && isq) ? 0u : (unsigned)hf * 8 * ld2, sstep = 16 * ld2, wrap = so0 + 28 * sstep;
    unsigned so = so0 + (unsigned)rot * sstep;
    lds += lds0;
    if (act) {
#pragma unroll 1
      for (int g = 0; g < 4; ++g) {
#pragma unroll
        for (int j = 0; j < 7; ++j) { bdma(lds, vo, rs, so); lds += lstep; so += sstep; }
        if (so >= wrap) so -= 28 * sstep;
      }
    }
  };
  const int ndw = isLN ? 4 : 6;
  if (w < ndw) {
    if (!isP) { if (w == 0) stream(0); stream(w < 2 ? 4 + w : w); }
    else stream(w);
  } else if (isLN) {
    __builtin_amdgcn_s_setprio(3);
#pragma unroll 1
    for (int pass = 1; pass < 2; ++pass) {
    u32x4 xc[4];
    if (pass) {
#pragma unroll
      for (int i = 0; i < 4; ++i) { PIN(xr[i]); xc[i] = xr[i]; }
      STAMPW(13, 4);
    } else {
#pragma unroll
      for (int i = 0; i < 4; ++i) { xc[i] = (u32x4){1u, 2u, 3u, (unsigned)lane}; PIN(xc[i]); }
    }
    float f[32];
#pragma unroll
    for (int i = 0; i < 4; ++i)
#pragma unroll
      for (int e = 0; e < 4; ++e) { f[8 * i + 2 * e] = bflo(xc[i][e]); f[8 * i + 2 * e + 1] = bfhi(xc[i][e]); }
    float s0 = 0.f, s1 = 0.f, s2 = 0.f, s3 = 0.f;
#pragma unroll
    for (int e = 0; e < 8; ++e) { s0 += f[e]; s1 += f[8 + e]; s2 += f[16 + e]; s3 += f[24 + e]; }
    const float mean = row_sum((s0 + s1) + (s2 + s3)) * (1.f / 448.f);
    const bool tail = lpart < 8;
    s0 = 0.f; s1 = 0.f; s2 = 0.f; s3 = 0.f;
#pragma unroll
    for (int e = 0; e < 8; ++e) { const float d0 = f[e] - mean, d1 = f[8 + e] - mean, d2 = f[16 + e] - mean, d3 = f[24 + e] - mean;
                                  s0 = fmaf(d0, d0, s0); s1 = fmaf(d1, d1, s1); s2 = fmaf(d2, d2, s2); s3 = fmaf(d3, d3, s3); }
    const float var = row_sum((s0 + s1) + (s2 + (tail ? s3 : 0.f))) * (1.f / 448.f);
    const float mb = bfr(mean);
    const float den = bfr(sqrtf(bfr(bfr(var) + 1e-5f)));
    const float rd = 1.f / den;
    const unsigned xo = WS_XF + (unsigned)(lne >> 1) * 28672 + (unsigned)((lne & 1) * 16 + (w - 4) * 4 + lrow) * 16 + (unsigned)((lpart >> 1) * 1024 + (lpart & 1) * 512);
#pragma unroll
    for (int i = 0; i < 4; ++i) {
      float y[8];
#pragma unroll
      for (int e = 0; e < 8; ++e) { const float a = bfr(f[8 * i + e] - mb); const float q0 = a * rd; y[e] = fmaf(fmaf(-q0, den, a), rd, q0); }
      u32x4 o = {pk2(y[0], y[1]), pk2(y[2], y[3]), pk2(y[4], y[5]), pk2(y[6], y[7])}; PIN(o);
      if (pass && (i < 3 || tail)) ST128(o, rs_ws, xo, (unsigned)i * 8192);
    }
    }
    STAMPW(14, 4);
    WAIT_VM(0);
    STAMPW(16, 4);
    if (lane == 0) set_flag(ws, FL_LN + lne * 4 + (w - 4), epoch);
    __builtin_amdgcn_s_setprio(0);
  }
  const int tbg = r * 8 + w;
  if (w < ndw) { WAIT_VM(0); STAMP(21); }
  if (w == 7) {
    const int idx = FL_LN + r * 64 + lane;
    if (isLN) { __builtin_amdgcn_s_sleep(LNSLEEP_A); } else { __builtin_amdgcn_s_sleep(LNSLEEP_B); }
    for (unsigned spins = 0; spins < SPIN_CAP; ++spins) {
      if (__all(flag_ld(ws, idx) == epoch)) break;
      __builtin_amdgcn_s_sleep(4);
    }
    __builtin_amdgcn_fence(__ATOMIC_ACQUIRE, "wavefront");
    STAMPW(1, 7);
  }
  BAR();
  u32x4 xf[28];
  const unsigned xvo = l16 + WS_XF + (unsigned)tbg * 28672;
#define XLOAD(j) xf[j] = LD128(rs_ws, xvo, (unsigned)((((j) + rot >= 28) ? (j) + rot - 28 : (j) + rot) * 1024))
#pragma unroll
  for (int j = 0; j < 10; ++j) XLOAD(j);
  STAMP(2);
  const int head = xcd + 8 * (ii >> 4), qb = ii & 15;
  int kvidx = -1;
  {
    int tb, cc;
    if (lane < 4) { tb = 4 * qb + lane; cc = 24 + (head >> 1); }
    else if (lane < 16) { tb = 4 * qb - 8 + (lane - 4); cc = head; }
    else { tb = 4 * qb - 8 + (lane - 16); cc = 16 + (head >> 1); }
    if (tb < 0) tb = 0;
    if (lane < 28) kvidx = FL_Q + ((tb >> 3) * 32 + cc) * 8 + (tb & 7);
  }
  const int kt0 = qb * 8 - 16;
  bf16x8 qf;
  auto issue_kv = [&]() {
#pragma unroll
    for (int j = 0; j < 6; ++j) {
      const int i = w * 6 + j;
      if (w < 4) { int kt = kt0 + i; if (kt < 0) kt = 0; bdma_sc1(lds0 + i * 1024, l16, rs_ws, WS_KF + (unsigned)(head * 128 + kt) * 1024); }
      else { const int jj = i - 24; int kb = (kt0 >> 1) + (jj >> 1); if (kb < 0) kb = 0;
             bdma_sc1(lds0 + i * 1024, l16, rs_ws, WS_VF + (unsigned)((head * 64 + kb) * 2 + (jj & 1)) * 1024); }
    }
    qf = BC8(LD128(rs_ws, l16, WS_QF + (unsigned)(head * 128 + qb * 8 + w) * 1024));
  };
  {
    auto epi_qk = [&](f32x16& acc, int t) {
      const int hd = t & 15;
      if (h == 1) { acc[12] = 0.f; acc[13] = 0.f; acc[14] = 0.f; acc[15] = 0.f; }
      u32x4 p0 = {pk2(acc[0], acc[1]), pk2(acc[2], acc[3]), pk2(acc[4], acc[5]), pk2(acc[6], acc[7])};
      u32x4 p1 = {pk2(acc[8], acc[9]), pk2(acc[10], acc[11]), pk2(acc[12], acc[13]), pk2(acc[14], acc[15])};
      const unsigned vo = (unsigned)(((m >> 4) * 64 + (m & 15) + 16 * h) * 16);
      const unsigned so = (t < 16 ? WS_QF : WS_KF) + (unsigned)(hd * 128 + tbg * 2) * 1024;
      ST128(p0, rs_ws, vo, so); ST128(p1, rs_ws, vo + 512, so);
    };
    auto epi_v = [&](f32x16& acc, int t) {
      const int hd = t - 32, shift = (t == 47) ? 4 : 0;
      const int d = (m - shift) & 31;
      if (d >= 28) {
#pragma unroll
        for (int i = 0; i < 16; ++i) acc[i] = (d == 28) ? 1.f : 0.f;
      }
      u32x4 p0 = {pk2(acc[0], acc[1]), pk2(acc[2], acc[3]), pk2(acc[8], acc[9]), pk2(acc[10], acc[11])};
      u32x4 p1 = {pk2(acc[4], acc[5]), pk2(acc[6], acc[7]), pk2(acc[12], acc[13]), pk2(acc[14], acc[15])};
      const unsigned vo = (unsigned)(((d >> 4) * 64 + (d & 15) + 16 * h) * 16);
      const unsigned so = WS_VF + (unsigned)(hd * 64 + tbg) * 2048;
      ST128(p0, rs_ws, vo, so); ST128(p1, rs_ws, vo + 512, so);
    };
    auto epi_h = [&](const f32x16& acc, int fs) {
      u32x4 p0;
#pragma unroll
      for (int e = 0; e < 4; ++e) p0[e] = pk2(acc[2 * e] * acc[8 + 2 * e], acc[2 * e + 1] * acc[9 + 2 * e]);
      ST128(p0, rs_ws, l16, WS_HF + (unsigned)(tbg * 112 + fs) * 1024);
    };
    f32x16 accA, accB, accC;
#pragma unroll
    for (int i = 0; i < 16; ++i) { accA[i] = 0.f; accB[i] = 0.f; accC[i] = 0.f; }
#define PASS3(PA, SA, TA, PB, SB, TB, PC, SC_, TC, SWAP) do { bf16x8 fa[2], fb[2], fc[2]; \
      _Pragma("unroll") for (int s = 0; s < 2; ++s) { fa[s] = tr_frag(PA, s * (SA), s * (SA) + (TA)); fb[s] = tr_frag(PB, s * (SB), s * (SB) + (TB)); fc[s] = tr_frag(PC, s * (SC_), s * (SC_) + (TC)); } \
      _Pragma("unroll") for (int s = 0; s < 28; ++s) { \
        accC = MFMA32(fc[s & 1], BC8(xf[s]), accC); \
        if (s + 2 < 28) fc[s & 1] = tr_frag(PC, (s + 2) * (SC_), (s + 2) * (SC_) + (TC)); \
        if (s + 10 < 28) XLOAD(s + 10); \
        SBAR(); \
        if (SWAP) accA = MFMA32(BC8(xf[s]), fa[s & 1], accA); else accA = MFMA32(fa[s & 1], BC8(xf[s]), accA); \
        if (s + 2 < 28) fa[s & 1] = tr_frag(PA, (s + 2) * (SA), (s + 2) * (SA) + (TA)); \
        SBAR(); \
        if (SWAP) accB = MFMA32(BC8(xf[s]), fb[s & 1], accB); else accB = MFMA32(fb[s & 1], BC8(xf[s]), accB); \
        if (s + 2 < 28) fb[s & 1] = tr_frag(PB, (s + 2) * (SB), (s + 2) * (SB) + (TB)); \
        SBAR(); } } while (0)
#define PASS2(PA, PB, KS, TS, HOOK) do { bf16x8 fa[3], fb[3]; \
      _Pragma("unroll") for (int s = 0; s < 3; ++s) { fa[s] = tr_frag(PA, s * (KS), s * (KS) + (TS)); fb[s] = tr_frag(PB, s * (KS), s * (KS) + (TS)); } \
      _Pragma("unroll") for (int s = 0; s < 28; ++s) { \
        accA = MFMA32(fa[s % 3], BC8(xf[s]), accA); \
        if (s + 3 < 28) fa[s % 3] = tr_frag(PA, (s + 3) * (KS), (s + 3) * (KS) + (TS)); \
        SBAR(); \
        accB = MFMA32(fb[s % 3], BC8(xf[s]), accB); \
        if (s + 3 < 28) fb[s % 3] = tr_frag(PB, (s + 3) * (KS), (s + 3) * (KS) + (TS)); \
        SBAR(); \
        HOOK(s); } } while (0)
    bool kv_early = false;
    u64 fv = 0;
#define HOOK2(s) do { \
      if (s == 5) fv = flag_ld(ws, kvidx); \
      if (s == 10) { kv_early = __all(kvidx < 0 || fv == epoch); __builtin_amdgcn_fence(__ATOMIC_ACQUIRE, "wavefront"); if (kv_early) issue_kv(); } } while (0)
    if (!isP) {
      LAS char* ql = smem + h * 512 + q4 * 64 + blk * 32 + p4 * 8;
      const unsigned gu0 = (unsigned)((blk ? 104 : 48) * 1024 + h * 1024 + q4 * 128 + (((p4 >> 1) ^ (((q4 >> 1) & 1) << 2) ^ (blk << 1)) * 16) + (p4 & 1) * 8);
      LAS char* g0 = smem + gu0; LAS char* g1 = smem + (gu0 ^ 32u); LAS char* g2 = smem + (gu0 ^ 64u); LAS char* g3 = smem + (gu0 ^ 96u);
      PASS3(ql, 1024, 256, g0, 2048, 512, g1, 2048, 512, false);
      epi_qk(accA, tA); epi_h(accB, 4 * c); epi_h(accC, 4 * c + 1);
      STAMP(17);
      WAIT_VM(0); if (lane == 0) set_flag(ws, FL_Q + (r * 32 + c) * 8 + w, epoch);
      BAR();
#pragma unroll
      for (int i = 0; i < 16; ++i) { accA[i] = 0.f; accB[i] = 0.f; }
      PASS2(g2, g3, 2048, 512, HOOK2);
      epi_h(accA, 4 * c + 2); epi_h(accB, 4 * c + 3);
    } else {
      LAS char* qa = smem + h * 1024 + q4 * 128 + ((((q4 >> 1) & 1)) * 64) + blk * 32 + p4 * 8;
      LAS char* qb_ = smem + h * 1024 + q4 * 128 + ((1 ^ ((q4 >> 1) & 1)) * 64) + blk * 32 + p4 * 8;
      const unsigned gu0 = (unsigned)((blk ? 100480 : 57344) + h * 768 + q4 * 96 + p4 * 8);
      LAS char* g0 = smem + gu0; LAS char* g1 = smem + gu0 + 32; LAS char* g2 = smem + gu0 + 64;
      if (pi < 8) {
        PASS3(qa, 2048, 512, qb_, 2048, 512, g0, 1536, 384, true);
        epi_v(accA, tA); epi_v(accB, tA + 1);
      } else {
        PASS3(qa, 2048, 512, qb_, 2048, 512, g0, 1536, 384, false);
        epi_qk(accA, tA); epi_qk(accB, tA + 1);
      }
      epi_h(accC, 64 + 3 * pi);
      STAMP(17);
      WAIT_VM(0); if (lane == 0) set_flag(ws, FL_Q + (r * 32 + c) * 8 + w, epoch);
      BAR();
#pragma unroll
      for (int i = 0; i < 16; ++i) { accA[i] = 0.f; accB[i] = 0.f; }
      PASS2(g1, g2, 1536, 384, HOOK2);
      epi_h(accA, 64 + 3 * pi + 1); epi_h(accB, 64 + 3 * pi + 2);
    }
    STAMP(20);
    if (kv_early) { WAIT_BAR(2); }
    else { poll_flags(ws, kvidx, epoch); issue_kv(); WAIT_BAR(0); }
  }
  STAMP(4);
  const bool hasD = ii < 28;
  const int tt64 = xcd * 4 + (ii & 3), ct64 = ii >> 2;
  const int tt = w & 1, kq = w >> 1;
  const int tbd = tt64 * 2 + tt, n0 = ct64 * 64, rt = tt64 >> 2;
  const rsrc_t rs_d = mk(wd, F_ * D_ * 2), rs_o = mk(wo, D_ * D_ * 2);
  const int ch8d = k8 ^ sw;
  const unsigned wvo = (unsigned)(r8 * (D_ * 2) + n0 * 2 + ch8d * 16);
#define DPAGE(s) ((s) < 14 ? 48 + 8 * (s) : ((s) < 20 ? 8 * ((s) - 14) : 48 + 8 * ((s) - 20)))
  const int rotd = 4 * ((ct64 + 2 * (ii & 3)) % 7);
#define RD(s) (((s) + rotd >= 28) ? (s) + rotd - 28 : (s) + rotd)
#define ISSUE_D(s) bdma(lds0 + (DPAGE(s) + w) * 1024, wvo, rs_d, (unsigned)(448 * (w >> 1) + 16 * RD(s) + 8 * (w & 1)) * (D_ * 2))
  if (hasD && w < 4) {
#pragma unroll
    for (int s = 0; s < 14; ++s) ISSUE_D(s);
  }
  u32x4 hf[28];
  bool h_early = false;
  int hidx = -1;
  if (lane < 28) { const int fs = 28 * kq + lane; const int cc = fs < 64 ? fs >> 2 : 16 + (fs - 64) / 3; hidx = FL_H + ((tbd >> 3) * 32 + cc) * 8 + (tbd & 7); }
  auto issue_h = [&]() {
#pragma unroll
    for (int s = 0; s < 28; ++s) hf[s] = LD128(rs_ws, l16, WS_HF + (unsigned)(tbd * 112 + kq * 28 + RD(s)) * 1024);
  };
  {
    const int mq = lane & 15, g = lane >> 4;
    const int t0 = w & ~1; const bool even = (w & 1) == 0;
    const float NEG = -__builtin_inff();
    f32x4 st[18];
    {
      f32x4 edge, diag;
#pragma unroll
      for (int e = 0; e < 4; ++e) { const int row = 4 * g + e; edge[e] = row > mq ? 0.f : NEG; diag[e] = row <= mq ? 0.f : NEG; }
      const f32x4 zero = {0.f, 0.f, 0.f, 0.f}, negv = {NEG, NEG, NEG, NEG};
      LAS char* kb = smem + t0 * 1024 + lane * 16;
      if (qb >= 2) {
#pragma unroll
        for (int T = 0; T < 18; ++T) {
          f32x4 cin = zero;
          if (T == 0) cin = even ? edge : negv;
          if (T == 1) cin = even ? zero : edge;
          if (T == 16) cin = even ? diag : zero;
          if (T == 17) cin = even ? negv : diag;
          st[T] = MFMA16(*(LAS bf16x8*)(kb + T * 1024), qf, cin);
        }
      } else {
#pragma unroll
        for (int T = 0; T < 18; ++T) {
          f32x4 cin = zero;
          if (T == 0) cin = even ? edge : negv;
          if (T == 1) cin = even ? zero : edge;
          if (T == 16) cin = even ? diag : zero;
          if (T == 17) cin = even ? negv : diag;
          if (kt0 + t0 + T < 0) cin = negv;
          st[T] = MFMA16(*(LAS bf16x8*)(kb + T * 1024), qf, cin);
        }
      }
    }
    const float SC = 0.18898223650461362f, L2E = 1.4426950408889634f;
    float mx = NEG;
#pragma unroll
    for (int T = 0; T < 18; ++T)
#pragma unroll
      for (int e = 0; e < 4; ++e) mx = fmaxf(mx, st[T][e]);
    { auto rr = __builtin_amdgcn_permlane16_swap(__float_as_uint(mx), __float_as_uint(mx), false, false); mx = fmaxf(__uint_as_float(rr[0]), __uint_as_float(rr[1])); }
    { auto rr = __builtin_amdgcn_permlane32_swap(__float_as_uint(mx), __float_as_uint(mx), false, false); mx = fmaxf(__uint_as_float(rr[0]), __uint_as_float(rr[1])); }
    const float C2 = SC * L2E;
    const float mc = -bfr(mx) * C2;
    if (hasD && w >= 4) {
#pragma unroll
      for (int s = 0; s < 14; ++s) ISSUE_D(s);
    }
    STAMP(5);
    f32x4 o0 = {0.f, 0.f, 0.f, 0.f}, o1 = {0.f, 0.f, 0.f, 0.f};
    u64 fvh = 0;
#pragma unroll
    for (int a = 0; a < 9; ++a) {
      if (a == 3) {
        if (hasD) { WAIT_VM(14); } else { WAIT_VM(0); }
        if (lane == 0) set_flag(ws, FL_H + (r * 32 + c) * 8 + w, epoch);
      }
      if (a == 7) fvh = flag_ld(ws, hidx);
      u32x4 pp;
#pragma unroll
      for (int hh = 0; hh < 2; ++hh)
#pragma unroll
        for (int e = 0; e < 2; ++e) {
          { unsigned r1 = pk2(st[2 * a + hh][2 * e], st[2 * a + hh][2 * e + 1]); PIN(r1);
          pp[2 * hh + e] = pk2(__builtin_amdgcn_exp2f(fmaf(bflo(r1), C2, mc)), __builtin_amdgcn_exp2f(fmaf(bfhi(r1), C2, mc))); }
        }
      LAS char* vb = smem + 24576 + ((t0 >> 1) + a) * 2048 + lane * 16;
      o0 = MFMA16(*(LAS bf16x8*)(vb), BC8(pp), o0);
      o1 = MFMA16(*(LAS bf16x8*)(vb + 1024), BC8(pp), o1);
    }
    h_early = hasD && __all(hidx < 0 || fvh == epoch);
    __builtin_amdgcn_fence(__ATOMIC_ACQUIRE, "wavefront");
    if (h_early) issue_h();
    const float l = __shfl(o1[0], 48 + mq);
    const float rl = 1.f / l;
    u32x4 ov;
    ov[0] = pk2(o0[0] * rl, o0[1] * rl); ov[1] = pk2(o0[2] * rl, o0[3] * rl);
    if (g == 3) { ov[2] = 0; ov[3] = 0; } else { ov[2] = pk2(o1[0] * rl, o1[1] * rl); ov[3] = pk2(o1[2] * rl, o1[3] * rl); }
    const int tok = w * 16 + mq;
    ST128(ov, rs_ws, (unsigned)((((tok >> 5) * 32 + (g >> 1)) * 64 + (tok & 31) + 32 * (g & 1)) * 16), WS_OF + (unsigned)((qb * 4 * 16 + head) * 2) * 1024);
  }
  STAMP(6);
  if (!hasD) {
    WAIT_VM(0);
    if (lane == 0) set_flag(ws, FL_O + (head * 16 + qb) * 8 + w, epoch);
    if (bid == 255 && w == 0) {
#pragma unroll 1
      for (int k = 0; k < 4; ++k) poll_flags(ws, FL_H + (k * 64 + lane) * 8, epoch);
      if (lane == 0) __hip_atomic_store((GAS u64*)(ws + WS_FL), epoch, __ATOMIC_RELAXED, __HIP_MEMORY_SCOPE_AGENT);
    }
    return;
  }
  {
    if (!h_early) { poll_flags(ws, hidx, epoch); issue_h(); }
    WAIT_BAR_L(29);
    STAMP(7);
#pragma unroll
    for (int s = 14; s < 20; ++s) ISSUE_D(s);
    const size_t rowoff = (size_t)(tbd * 32 + m) * D_ + n0 + (kq >> 1) * 32 + 16 * (kq & 1) + 4 * h;
    const u32x2 xv0 = *(const u32x2*)(x + rowoff), xv1 = *(const u32x2*)(x + rowoff + 8);
    f32x16 y0, y1, s0, s1;
#pragma unroll
    for (int i = 0; i < 16; ++i) { y0[i] = 0.f; y1[i] = 0.f; s0[i] = 0.f; s1[i] = 0.f; }
    const unsigned wlo = (unsigned)(kq * 2048 + (4 * h + q4) * 128 + blk * 32 + p4 * 8);
    LAS char* wl0 = smem + wlo + (((q4 >> 1) & 1) * 64);
    LAS char* wl1 = smem + wlo + ((1 ^ ((q4 >> 1) & 1)) * 64);
#define DF0(s) tr_frag(wl0, DPAGE(s) * 1024, DPAGE(s) * 1024 + 1024)
#define DF1(s) tr_frag(wl1, DPAGE(s) * 1024, DPAGE(s) * 1024 + 1024)
#define D_RANGE(S0, S1) do { bf16x8 fa[3], fb[3]; \
      _Pragma("unroll") for (int s = 0; s < 3; ++s) if ((S0) + s < (S1)) { fa[s] = DF0((S0) + s); fb[s] = DF1((S0) + s); } \
      _Pragma("unroll") for (int s = (S0); s < (S1); ++s) { \
        y0 = MFMA32(fa[(s - (S0)) % 3], BC8(hf[s]), y0); if (s + 3 < (S1)) fa[(s - (S0)) % 3] = DF0(s + 3); SBAR(); \
        y1 = MFMA32(fb[(s - (S0)) % 3], BC8(hf[s]), y1); if (s + 3 < (S1)) fb[(s - (S0)) % 3] = DF1(s + 3); SBAR(); } PIN(y0); PIN(y1); } while (0)
    D_RANGE(0, 8);
    WAIT_BAR(6);
    if (lane == 0) set_flag(ws, FL_O + (head * 16 + qb) * 8 + w, epoch);
#pragma unroll
    for (int s = 20; s < 28; ++s) ISSUE_D(s);
    const int oidx = lane < 8 ? FL_O + ((4 * kq + (lane >> 1)) * 16 + (tbd >> 2)) * 8 + 2 * (tbd & 3) + (lane & 1) : -1;
    D_RANGE(8, 14);
    WAIT_BAR(8);
    STAMP(8);
    {
#pragma unroll
      for (int j = 0; j < 6; ++j) {
        const int n = w * 6 + j, kk = n >> 4, i = n & 15;
        int row = 112 * kk + 8 * i + r8; if (row > 447) row = 447;
        bdma(lds0 + (112 + n) * 1024, (unsigned)(row * (D_ * 2) + n0 * 2 + ch8d * 16), rs_o, 0u);
      }
    }
    const u64 fvo = flag_ld(ws, oidx);
    D_RANGE(14, 16);
    BAR();
    {
#pragma unroll
      for (int j = 0; j < 2; ++j) {
        const int i = w * 2 + j;
        int row = 336 + 8 * i + r8; if (row > 447) row = 447;
        bdma(lds0 + i * 1024, (unsigned)(row * (D_ * 2) + n0 * 2 + ch8d * 16), rs_o, 0u);
      }
    }
    if (!__all(oidx < 0 || fvo == epoch)) poll_flags(ws, oidx, epoch);
    __builtin_amdgcn_fence(__ATOMIC_ACQUIRE, "wavefront");
    u32x4 of[8];
#pragma unroll
    for (int s = 0; s < 8; ++s) of[s] = LD128(rs_ws, l16, WS_OF + (unsigned)((tbd * 16 + kq * 4) * 2 + s) * 1024);
    D_RANGE(16, 20);
    WAIT_BAR(16);
    STAMP(9);
    D_RANGE(20, 28);
    WAIT_BAR(8);
    STAMP(10);
    {
      LAS char* base = smem + (kq < 3 ? (112 + 16 * kq) * 1024 : 0) + blk * 32 + p4 * 8;
#pragma unroll
      for (int hd = 0; hd < 4; ++hd)
#pragma unroll
        for (int s = 0; s < 2; ++s) {
          const int rb = 28 * hd + 8 * s;
          const int row0 = rb + 4 * h + q4, row1 = row0 + 16;
          const int o00 = (row0 >> 3) * 1024 + (row0 & 7) * 128, o01 = (row1 >> 3) * 1024 + (row1 & 7) * 128;
          const int x0 = ((row0 >> 1) & 1) * 64, x1 = ((row1 >> 1) & 1) * 64;
          const bf16x8 a0 = tr_frag(base, o00 + x0, o01 + x1);
          const bf16x8 a1 = tr_frag(base, o00 + (x0 ^ 64), o01 + (x1 ^ 64));
          s0 = MFMA32(a0, BC8(of[hd * 2 + s]), s0);
          s1 = MFMA32(a1, BC8(of[hd * 2 + s]), s1);
        }
    }
    PIN(s0); PIN(s1);
    LBAR();
    STAMP(11);
    {
      LAS f32x4* red = (LAS f32x4*)smem;
#pragma unroll
      for (int qd = 0; qd < 4; ++qd) {
        if (qd != kq) {
          const f32x16& yy = (qd >> 1) ? y1 : y0; const f32x16& ss = (qd >> 1) ? s1 : s0;
          const int b = 8 * (qd & 1);
          LAS f32x4* dst = red + (size_t)(((tt * 4 + kq) * 4 + qd) * 4) * 64 + lane;
          dst[0] = (f32x4){yy[b], yy[b + 1], yy[b + 2], yy[b + 3]}; dst[64] = (f32x4){yy[b + 4], yy[b + 5], yy[b + 6], yy[b + 7]};
          dst[128] = (f32x4){ss[b], ss[b + 1], ss[b + 2], ss[b + 3]}; dst[192] = (f32x4){ss[b + 4], ss[b + 5], ss[b + 6], ss[b + 7]};
        }
      }
      LBAR();
      f32x4 ya, yb, sa, sb;
      {
        const bool c1 = (kq >> 1) != 0, hh = (kq & 1) != 0;
#pragma unroll
        for (int e = 0; e < 4; ++e) {
          const float ylo = c1 ? y1[e] : y0[e], yhi = c1 ? y1[8 + e] : y0[8 + e], ylo2 = c1 ? y1[4 + e] : y0[4 + e], yhi2 = c1 ? y1[12 + e] : y0[12 + e];
          const float slo = c1 ? s1[e] : s0[e], shi = c1 ? s1[8 + e] : s0[8 + e], slo2 = c1 ? s1[4 + e] : s0[4 + e], shi2 = c1 ? s1[12 + e] : s0[12 + e];
          ya[e] = hh ? yhi : ylo; yb[e] = hh ? yhi2 : ylo2; sa[e] = hh ? shi : slo; sb[e] = hh ? shi2 : slo2;
        }
      }
#pragma unroll
      for (int k = 1; k < 4; ++k) {
        const int src = (kq + k) & 3;
        LAS f32x4* p = red + (size_t)(((tt * 4 + src) * 4 + kq) * 4) * 64 + lane;
        ya += p[0]; yb += p[64]; sa += p[128]; sb += p[192];
      }
#pragma unroll
      for (int i = 0; i < 2; ++i) {
        const f32x4 yv = i ? yb : ya, sv = i ? sb : sa;
        const u32x2 xv = i ? xv1 : xv0;
        float o[4];
#pragma unroll
        for (int e = 0; e < 4; ++e) {
          const float yy = yv[e];
          const float sg = yy * __builtin_amdgcn_rcpf(1.f + __builtin_amdgcn_exp2f(-yy * 1.4426950408889634f));
          const float xe = (e & 1) ? bfhi(xv[e >> 1]) : bflo(xv[e >> 1]);
          o[e] = xe + sv[e] * sg;
        }
        u32x2 ov = {pk2(o[0], o[1]), pk2(o[2], o[3])};
        *(u32x2*)(out + rowoff + 8 * i) = ov;
      }
    }
  }
  STAMP(12);
}

extern "C" void kernel_launch(void* const* d_in, const int* in_sizes, int n_in, void* d_out, int out_size, void* d_ws, size_t ws_size,
                              hipStream_t stream) {
  (void)hipFuncSetAttribute((const void*)mega, hipFuncAttributeMaxDynamicSharedMemorySize, 163840);
  mega<<<256, 512, 163840, stream>>>((const u16*)d_in[0], (const u16*)d_in[1], (const u16*)d_in[2], (const u16*)d_in[4], (const u16*)d_in[5],
                                     (const u16*)d_in[6], (u16*)d_out, (char*)d_ws);
}
```

```cpp
#include <hip/hip_runtime.h>
#include <hip/hip_bf16.h>
#include <stdint.h>

#define LAS __attribute__((address_space(3)))
#define GAS __attribute__((address_space(1)))
#define DI __device__ __forceinline__

typedef unsigned short u16;
typedef unsigned long long u64;
typedef __bf16 bf16x8 __attribute__((ext_vector_type(8)));
typedef __bf16 bf16x2 __attribute__((ext_vector_type(2)));
typedef short s16x4 __attribute__((ext_vector_type(4)));
typedef short s16x8 __attribute__((ext_vector_type(8)));
typedef float f32x4 __attribute__((ext_vector_type(4)));
typedef float f32x16 __attribute__((ext_vector_type(16)));
typedef unsigned u32x4 __attribute__((ext_vector_type(4)));
typedef unsigned u32x2 __attribute__((ext_vector_type(2)));
typedef __amdgpu_buffer_rsrc_t rsrc_t;

constexpr int S_ = 2048, D_ = 448, F_ = 1792, NQKV = 1344;
constexpr unsigned WS_QF = 0, WS_KF = 2u << 20, WS_VF = 4u << 20, WS_OF = 6u << 20, WS_HF = 8u << 20, WS_XF = 16u << 20, WS_FL = 18u << 20, WS_BYTES = 19u << 20;
constexpr unsigned FL_LN = 0, FL_Q = 512, FL_H = 2560, FL_O = 4608;
constexpr unsigned SPIN_CAP = 1u << 16;

#define LNSLEEP_A 8
#define LNSLEEP_B 70
#define XPF 16
#define SBAR() __builtin_amdgcn_sched_barrier(0)
#define PIN(x) asm volatile("" : "+v"(x))
#define WAIT_VM(N) asm volatile("s_waitcnt vmcnt(" #N ")" ::: "memory")
#define BAR() asm volatile("s_barrier" ::: "memory")
#define WAIT_BAR(N) asm volatile("s_waitcnt vmcnt(" #N ")\n\ts_barrier" ::: "memory")
#define WAIT_BAR_L(N) asm volatile("s_waitcnt vmcnt(" #N ") lgkmcnt(0)\n\ts_barrier" ::: "memory")
#define LBAR() asm volatile("s_waitcnt lgkmcnt(0)\n\ts_barrier" ::: "memory")

DI rsrc_t mk(const void* p, unsigned bytes) { return __builtin_amdgcn_make_buffer_rsrc((void*)p, (short)0, (int)bytes, 0x00020000); }
DI void bdma(unsigned ldsaddr, unsigned voff, rsrc_t rs, unsigned soff) {
  asm volatile("s_mov_b32 m0, %0\n\ts_nop 0\n\tbuffer_load_dwordx4 %1, %2, %3 offen lds" :: "s"(ldsaddr), "v"(voff), "s"(rs), "s"(soff) : "memory");
}
DI void bdma_sc1(unsigned ldsaddr, unsigned voff, rsrc_t rs, unsigned soff) {
  asm volatile("s_mov_b32 m0, %0\n\ts_nop 0\n\tbuffer_load_dwordx4 %1, %2, %3 offen sc1 lds" :: "s"(ldsaddr), "v"(voff), "s"(rs), "s"(soff) : "memory");
}
DI bf16x8 tr_frag(LAS char* p, int off0, int off1) {
  s16x4 lo = __builtin_amdgcn_ds_read_tr16_b64_v4i16((LAS s16x4*)(p + off0));
  s16x4 hi = __builtin_amdgcn_ds_read_tr16_b64_v4i16((LAS s16x4*)(p + off1));
  s16x8 v = __builtin_shufflevector(lo, hi, 0, 1, 2, 3, 4, 5, 6, 7);
  return __builtin_bit_cast(bf16x8, v);
}
DI unsigned pk2(float a, float b) { bf16x2 c; c[0] = (__bf16)a; c[1] = (__bf16)b; return __builtin_bit_cast(unsigned, c); }
DI float bflo(unsigned u) { return __uint_as_float(u << 16); }
DI float bfhi(unsigned u) { return __uint_as_float(u & 0xffff0000u); }
DI float bfr(float a) { return bflo(pk2(a, 0.f)); }
#define MFMA32(a, b, c) __builtin_amdgcn_mfma_f32_32x32x16_bf16((a), (b), (c), 0, 0, 0)
#define MFMA16(a, b, c) __builtin_amdgcn_mfma_f32_16x16x32_bf16((a), (b), (c), 0, 0, 0)
#define ST128(v, rs, voff, soff) __builtin_amdgcn_raw_buffer_store_b128((v), (rs), (voff) + (soff), 0, 16)
#define LD128(rs, voff, soff) __builtin_amdgcn_raw_buffer_load_b128((rs), (voff), (soff), 16)
#define BC8(v) __builtin_bit_cast(bf16x8, v)

#define STAMPW(i, WV)
#define STAMP(i) STAMPW(i, 0)

template <int CTRL> DI float dpp_add(float v) {
  return v + __builtin_bit_cast(float, __builtin_amdgcn_update_dpp(0, __builtin_bit_cast(int, v), CTRL, 0xf, 0xf, true));
}
DI float wave_sum(float v) {
  v = dpp_add<0x111>(v); v = dpp_add<0x112>(v); v = dpp_add<0x114>(v); v = dpp_add<0x118>(v);
  const float r0 = __builtin_bit_cast(float, __builtin_amdgcn_readlane(__builtin_bit_cast(int, v), 15));
  const float r1 = __builtin_bit_cast(float, __builtin_amdgcn_readlane(__builtin_bit_cast(int, v), 31));
  const float r2 = __builtin_bit_cast(float, __builtin_amdgcn_readlane(__builtin_bit_cast(int, v), 47));
  const float r3 = __builtin_bit_cast(float, __builtin_amdgcn_readlane(__builtin_bit_cast(int, v), 63));
  return (r0 + r1) + (r2 + r3);
}
DI float row_sum(float v) {
  v = dpp_add<0x124>(v); v = dpp_add<0x128>(v);
  { auto rr = __builtin_amdgcn_permlane16_swap(__float_as_uint(v), __float_as_uint(v), false, false); v = __uint_as_float(rr[0]) + __uint_as_float(rr[1]); }
  { auto rr = __builtin_amdgcn_permlane32_swap(__float_as_uint(v), __float_as_uint(v), false, false); v = __uint_as_float(rr[0]) + __uint_as_float(rr[1]); }
  return v;
}
DI GAS u64* flag_ptr(char* ws, int idx) { return (GAS u64*)(ws + WS_FL + 256 + (size_t)idx * 8); }
DI u64 flag_ld(char* ws, int idx) { return __hip_atomic_load(flag_ptr(ws, idx < 0 ? 0 : idx), __ATOMIC_RELAXED, __HIP_MEMORY_SCOPE_AGENT); }
DI void poll_flags(char* ws, int idx, u64 epoch) {
  for (unsigned spins = 0; spins < SPIN_CAP; ++spins) {
    const u64 v = flag_ld(ws, idx);
    if (__all(idx < 0 || v == epoch)) break;
    __builtin_amdgcn_s_sleep(3);
  }
  __builtin_amdgcn_fence(__ATOMIC_ACQUIRE, "wavefront");
}
DI void set_flag(char* ws, int idx, u64 epoch) { __hip_atomic_store(flag_ptr(ws, idx), epoch, __ATOMIC_RELAXED, __HIP_MEMORY_SCOPE_AGENT); }

__global__ __launch_bounds__(512, 2) void mega(const u16* __restrict__ x, const u16* __restrict__ wqkv, const u16* __restrict__ wo,
                                              const u16* __restrict__ wg, const u16* __restrict__ wu, const u16* __restrict__ wd,
                                              u16* __restrict__ out, char* __restrict__ ws) {
  extern __shared__ __attribute__((aligned(16))) char smem_[];
  LAS char* smem = (LAS char*)smem_;
  const unsigned lds0 = (unsigned)(uintptr_t)smem;
  const int tid = threadIdx.x, lane = tid & 63, w = __builtin_amdgcn_readfirstlane(tid >> 6);
  const int bid = blockIdx.x, xcd = bid & 7, ii = bid >> 3;
  const int m = lane & 31, h = lane >> 5;
  const int blk = (lane >> 4) & 1, q4 = (lane & 15) >> 2, p4 = lane & 3;
  const rsrc_t rs_ws = mk(ws, WS_BYTES);
  const unsigned l16 = (unsigned)lane * 16;
  STAMP(0);
  const int r = (xcd & 1) * 4 + (ii & 3), c = (xcd >> 1) * 8 + (ii >> 2);
  const bool isP = c >= 16;
  const int pi = c - 16;
  const int tA = isP ? (pi < 8 ? 32 + 2 * pi : 2 * (pi - 8)) : 16 + c;
  const int rot = 7 * ((ii + (ii >> 2)) & 3);
  const bool isLN = xcd < 4;
  const int lne = ii * 4 + xcd;
  const int lpart = lane >> 2, lrow = lane & 3;
  u32x4 xr[4];
#pragma unroll
  for (int i = 0; i < 4; ++i) xr[i] = (u32x4){0, 0, 0, 0};
  if (isLN && w >= 4) {
    const u16* xp = x + (size_t)(lne * 16 + (w - 4) * 4 + lrow) * D_ + lpart * 8;
#pragma unroll
    for (int i = 0; i < 4; ++i) if (i < 3 || lpart < 8) xr[i] = *(const u32x4*)(xp + i * 128);
  }
  const u64 epoch = __hip_atomic_load((GAS u64*)(ws + WS_FL), __ATOMIC_RELAXED, __HIP_MEMORY_SCOPE_AGENT) + 1;
  const int r8 = lane >> 3, k8 = lane & 7, sw = ((r8 >> 1) & 1) << 2;
  const unsigned scr = isP ? 144u * 1024 : 28u * 1024;
  auto stream = [&](int kind) {
    const int hf = kind & 1;
    const bool isq = kind < 2;
    const u16* base = isq ? wqkv : (kind < 4 ? wg : wu);
    const rsrc_t rs = mk(base, isq ? D_ * NQKV * 2 : D_ * F_ * 2);
    const unsigned ld2 = isq ? NQKV * 2 : F_ * 2;
    unsigned vo, lds, lstep; bool act = true;
    if (!isP) {
      if (isq) { vo = (unsigned)((lane >> 2) * (NQKV * 2) + 56 * tA + (lane & 3) * 16); lds = 0; lstep = 1024; }
      else { vo = (unsigned)(r8 * (F_ * 2) + 128 * c + (k8 ^ sw ^ (kind < 4 ? 0 : 2)) * 16); lds = (unsigned)((kind < 4 ? 48 : 104) + hf) * 1024; lstep = 2048; }
    } else {
      if (isq) { int cB = 56 * (tA + 1); if (cB > 2624) cB = 2624; const int ch8 = k8 ^ sw;
                 vo = (unsigned)(r8 * (NQKV * 2) + (ch8 < 4 ? 56 * tA + 16 * ch8 : cB + 16 * (ch8 - 4))); lds = (unsigned)hf * 1024; lstep = 2048; }
      else { const int r6 = lane / 6, k6 = lane - 6 * r6; vo = (unsigned)(r6 * (F_ * 2) + 2 * (1024 + 48 * pi) + 16 * k6);
             lds = (kind < 4 ? 57344u : 100480u) + (unsigned)hf * 768; lstep = 1536; act = lane < 48; }
    }
    const unsigned so0 = (!isP && isq) ? 0u : (unsigned)hf * 8 * ld2, sstep = 16 * ld2, wrap = so0 + 28 * sstep;
    unsigned so = so0 + (unsigned)rot * sstep;
    lds += lds0;
    if (act) {
#pragma unroll 1
      for (int g = 0; g < 4; ++g) {
#pragma unroll
        for (int j = 0; j < 7; ++j) { bdma(lds, vo, rs, so); lds += lstep; so += sstep; }
        if (so >= wrap) so -= 28 * sstep;
      }
    }
  };
  const int ndw = isLN ? 4 : 6;
  if (w < ndw) {
    if (!isP) { if (w == 0) stream(0); stream(w < 2 ? 4 + w : w); }
    else stream(w);
  } else if (isLN) {
    __builtin_amdgcn_s_setprio(3);
#pragma unroll 1
    for (int pass = 1; pass < 2; ++pass) {
    u32x4 xc[4];
    if (pass) {
#pragma unroll
      for (int i = 0; i < 4; ++i) { PIN(xr[i]); xc[i] = xr[i]; }
      STAMPW(13, 4);
    } else {
#pragma unroll
      for (int i = 0; i < 4; ++i) { xc[i] = (u32x4){1u, 2u, 3u, (unsigned)lane}; PIN(xc[i]); }
    }
    float f[32];
#pragma unroll
    for (int i = 0; i < 4; ++i)
#pragma unroll
      for (int e = 0; e < 4; ++e) { f[8 * i + 2 * e] = bflo(xc[i][e]); f[8 * i + 2 * e + 1] = bfhi(xc[i][e]); }
    float s0 = 0.f, s1 = 0.f, s2 = 0.f, s3 = 0.f;
#pragma unroll
    for (int e = 0; e < 8; ++e) { s0 += f[e]; s1 += f[8 + e]; s2 += f[16 + e]; s3 += f[24 + e]; }
    const float mean = row_sum((s0 + s1) + (s2 + s3)) * (1.f / 448.f);
    const bool tail = lpart < 8;
    s0 = 0.f; s1 = 0.f; s2 = 0.f; s3 = 0.f;
#pragma unroll
    for (int e = 0; e < 8; ++e) { const float d0 = f[e] - mean, d1 = f[8 + e] - mean, d2 = f[16 + e] - mean, d3 = f[24 + e] - mean;
                                  s0 = fmaf(d0, d0, s0); s1 = fmaf(d1, d1, s1); s2 = fmaf(d2, d2, s2); s3 = fmaf(d3, d3, s3); }
    const float var = row_sum((s0 + s1) + (s2 + (tail ? s3 : 0.f))) * (1.f / 448.f);
    const float mb = bfr(mean);
    const float den = bfr(sqrtf(bfr(bfr(var) + 1e-5f)));
    const float rd = 1.f / den;
    const unsigned xo = WS_XF + (unsigned)(lne >> 1) * 28672 + (unsigned)((lne & 1) * 16 + (w - 4) * 4 + lrow) * 16 + (unsigned)((lpart >> 1) * 1024 + (lpart & 1) * 512);
#pragma unroll
    for (int i = 0; i < 4; ++i) {
      float y[8];
#pragma unroll
      for (int e = 0; e < 8; ++e) { const float a = bfr(f[8 * i + e] - mb); const float q0 = a * rd; y[e] = fmaf(fmaf(-q0, den, a), rd, q0); }
      u32x4 o = {pk2(y[0], y[1]), pk2(y[2], y[3]), pk2(y[4], y[5]), pk2(y[6], y[7])}; PIN(o);
      if (pass && (i < 3 || tail)) ST128(o, rs_ws, xo, (unsigned)i * 8192);
    }
    }
    STAMPW(14, 4);
    WAIT_VM(0);
    STAMPW(16, 4);
    if (lane == 0) set_flag(ws, FL_LN + lne * 4 + (w - 4), epoch);
    __builtin_amdgcn_s_setprio(0);
  }
  const int tbg = r * 8 + w;
  if (w < ndw) { WAIT_VM(0); STAMP(21); }
  BAR();
  if (w == 7) {
    poll_flags(ws, FL_LN + r * 64 + lane, epoch);
    STAMPW(1, 7);
  }
  BAR();
  u32x4 xf[28];
  const unsigned xvo = l16 + WS_XF + (unsigned)tbg * 28672;
#define XLOAD(j) xf[j] = LD128(rs_ws, xvo, (unsigned)((((j) + rot >= 28) ? (j) + rot - 28 : (j) + rot) * 1024))
#pragma unroll
  for (int j = 0; j < XPF; ++j) XLOAD(j);
  STAMP(2);
  const int head = xcd + 8 * (ii >> 4), qb = ii & 15;
  int kvidx = -1;
  {
    int tb, cc;
    if (lane < 4) { tb = 4 * qb + lane; cc = 24 + (head >> 1); }
    else if (lane < 16) { tb = 4 * qb - 8 + (lane - 4); cc = head; }
    else { tb = 4 * qb - 8 + (lane - 16); cc = 16 + (head >> 1); }
    if (tb < 0) tb = 0;
    if (lane < 28) kvidx = FL_Q + ((tb >> 3) * 32 + cc) * 8 + (tb & 7);
  }
  const int kt0 = qb * 8 - 16;
  bf16x8 qf;
  auto issue_kv = [&]() {
#pragma unroll
    for (int j = 0; j < 6; ++j) {
      const int i = w * 6 + j;
      if (w < 4) { int kt = kt0 + i; if (kt < 0) kt = 0; bdma_sc1(lds0 + i * 1024, l16, rs_ws, WS_KF + (unsigned)(head * 128 + kt) * 1024); }
      else { const int jj = i - 24; int kb = (kt0 >> 1) + (jj >> 1); if (kb < 0) kb = 0;
             bdma_sc1(lds0 + i * 1024, l16, rs_ws, WS_VF + (unsigned)((head * 64 + kb) * 2 + (jj & 1)) * 1024); }
    }
    qf = BC8(LD128(rs_ws, l16, WS_QF + (unsigned)(head * 128 + qb * 8 + w) * 1024));
  };
  {
    auto epi_qk = [&](f32x16& acc, int t) {
      const int hd = t & 15;
      if (h == 1) { acc[12] = 0.f; acc[13] = 0.f; acc[14] = 0.f; acc[15] = 0.f; }
      u32x4 p0 = {pk2(acc[0], acc[1]), pk2(acc[2], acc[3]), pk2(acc[4], acc[5]), pk2(acc[6], acc[7])};
      u32x4 p1 = {pk2(acc[8], acc[9]), pk2(acc[10], acc[11]), pk2(acc[12], acc[13]), pk2(acc[14], acc[15])};
      const unsigned vo = (unsigned)(((m >> 4) * 64 + (m & 15) + 16 * h) * 16);
      const unsigned so = (t < 16 ? WS_QF : WS_KF) + (unsigned)(hd * 128 + tbg * 2) * 1024;
      ST128(p0, rs_ws, vo, so); ST128(p1, rs_ws, vo + 512, so);
    };
    auto epi_v = [&](f32x16& acc, int t) {
      const int hd = t - 32, shift = (t == 47) ? 4 : 0;
      const int d = (m - shift) & 31;
      if (d >= 28) {
#pragma unroll
        for (int i = 0; i < 16; ++i) acc[i] = (d == 28) ? 1.f : 0.f;
      }
      u32x4 p0 = {pk2(acc[0], acc[1]), pk2(acc[2], acc[3]), pk2(acc[8], acc[9]), pk2(acc[10], acc[11])};
      u32x4 p1 = {pk2(acc[4], acc[5]), pk2(acc[6], acc[7]), pk2(acc[12], acc[13]), pk2(acc[14], acc[15])};
      const unsigned vo = (unsigned)(((d >> 4) * 64 + (d & 15) + 16 * h) * 16);
      const unsigned so = WS_VF + (unsigned)(hd * 64 + tbg) * 2048;
      ST128(p0, rs_ws, vo, so); ST128(p1, rs_ws, vo + 512, so);
    };
    auto epi_h = [&](const f32x16& acc, int fs) {
      u32x4 p0;
#pragma unroll
      for (int e = 0; e < 4; ++e) p0[e] = pk2(acc[2 * e] * acc[8 + 2 * e], acc[2 * e + 1] * acc[9 + 2 * e]);
      ST128(p0, rs_ws, l16, WS_HF + (unsigned)(tbg * 112 + fs) * 1024);
    };
    f32x16 accA, accB, accC;
#pragma unroll
    for (int i = 0; i < 16; ++i) { accA[i] = 0.f; accB[i] = 0.f; accC[i] = 0.f; }
#define PASS3(PA, SA, TA, PB, SB, TB, PC, SC_, TC, SWAP) do { bf16x8 fa[2], fb[2], fc[2]; \
      _Pragma("unroll") for (int s = 0; s < 2; ++s) { fa[s] = tr_frag(PA, s * (SA), s * (SA) + (TA)); fb[s] = tr_frag(PB, s * (SB), s * (SB) + (TB)); fc[s] = tr_frag(PC, s * (SC_), s * (SC_) + (TC)); } \
      _Pragma("unroll") for (int s = 0; s < 28; ++s) { \
        accC = MFMA32(fc[s & 1], BC8(xf[s]), accC); \
        if (s + 2 < 28) fc[s & 1] = tr_frag(PC, (s + 2) * (SC_), (s + 2) * (SC_) + (TC)); \
        if (s + XPF < 28) XLOAD(s + XPF); \
        SBAR(); \
        if (SWAP) accA = MFMA32(BC8(xf[s]), fa[s & 1], accA); else accA = MFMA32(fa[s & 1], BC8(xf[s]), accA); \
        if (s + 2 < 28) fa[s & 1] = tr_frag(PA, (s + 2) * (SA), (s + 2) * (SA) + (TA)); \
        SBAR(); \
        if (SWAP) accB = MFMA32(BC8(xf[s]), fb[s & 1], accB); else accB = MFMA32(fb[s & 1], BC8(xf[s]), accB); \
        if (s + 2 < 28) fb[s & 1] = tr_frag(PB, (s + 2) * (SB), (s + 2) * (SB) + (TB)); \
        SBAR(); } } while (0)
#define PASS2(PA, PB, KS, TS, HOOK) do { bf16x8 fa[3], fb[3]; \
      _Pragma("unroll") for (int s = 0; s < 3; ++s) { fa[s] = tr_frag(PA, s * (KS), s * (KS) + (TS)); fb[s] = tr_frag(PB, s * (KS), s * (KS) + (TS)); } \
      _Pragma("unroll") for (int s = 0; s < 28; ++s) { \
        accA = MFMA32(fa[s % 3], BC8(xf[s]), accA); \
        if (s + 3 < 28) fa[s % 3] = tr_frag(PA, (s + 3) * (KS), (s + 3) * (KS) + (TS)); \
        SBAR(); \
        accB = MFMA32(fb[s % 3], BC8(xf[s]), accB); \
        if (s + 3 < 28) fb[s % 3] = tr_frag(PB, (s + 3) * (KS), (s + 3) * (KS) + (TS)); \
        SBAR(); \
        HOOK(s); } } while (0)
    bool kv_early = false;
    u64 fv = 0;
#define HOOK2(s) do { \
      if (s == 5) fv = flag_ld(ws, kvidx); \
      if (s == 10) { kv_early = __all(kvidx < 0 || fv == epoch); __builtin_amdgcn_fence(__ATOMIC_ACQUIRE, "wavefront"); if (kv_early) issue_kv(); } } while (0)
    if (!isP) {
      LAS char* ql = smem + h * 512 + q4 * 64 + blk * 32 + p4 * 8;
      const unsigned gu0 = (unsigned)((blk ? 104 : 48) * 1024 + h * 1024 + q4 * 128 + (((p4 >> 1) ^ (((q4 >> 1) & 1) << 2) ^ (blk << 1)) * 16) + (p4 & 1) * 8);
      LAS char* g0 = smem + gu0; LAS char* g1 = smem + (gu0 ^ 32u); LAS char* g2 = smem + (gu0 ^ 64u); LAS char* g3 = smem + (gu0 ^ 96u);
      PASS3(ql, 1024, 256, g0, 2048, 512, g1, 2048, 512, false);
      epi_qk(accA, tA); epi_h(accB, 4 * c); epi_h(accC, 4 * c + 1);
      STAMP(17);
      WAIT_VM(0); if (lane == 0) set_flag(ws, FL_Q + (r * 32 + c) * 8 + w, epoch);
      BAR();
#pragma unroll
      for (int i = 0; i < 16; ++i) { accA[i] = 0.f; accB[i] = 0.f; }
      PASS2(g2, g3, 2048, 512, HOOK2);
      epi_h(accA, 4 * c + 2); epi_h(accB, 4 * c + 3);
    } else {
      LAS char* qa = smem + h * 1024 + q4 * 128 + ((((q4 >> 1) & 1)) * 64) + blk * 32 + p4 * 8;
      LAS char* qb_ = smem + h * 1024 + q4 * 128 + ((1 ^ ((q4 >> 1) & 1)) * 64) + blk * 32 + p4 * 8;
      const unsigned gu0 = (unsigned)((blk ? 100480 : 57344) + h * 768 + q4 * 96 + p4 * 8);
      LAS char* g0 = smem + gu0; LAS char* g1 = smem + gu0 + 32; LAS char* g2 = smem + gu0 + 64;
      if (pi < 8) {
        PASS3(qa, 2048, 512, qb_, 2048, 512, g0, 1536, 384, true);
        epi_v(accA, tA); epi_v(accB, tA + 1);
      } else {
        PASS3(qa, 2048, 512, qb_, 2048, 512, g0, 1536, 384, false);
        epi_qk(accA, tA); epi_qk(accB, tA + 1);
      }
      epi_h(accC, 64 + 3 * pi);
      STAMP(17);
      WAIT_VM(0); if (lane == 0) set_flag(ws, FL_Q + (r * 32 + c) * 8 + w, epoch);
      BAR();
#pragma unroll
      for (int i = 0; i < 16; ++i) { accA[i] = 0.f; accB[i] = 0.f; }
      PASS2(g1, g2, 1536, 384, HOOK2);
      epi_h(accA, 64 + 3 * pi + 1); epi_h(accB, 64 + 3 * pi + 2);
    }
    STAMP(20);
    if (!kv_early) { poll_flags(ws, kvidx, epoch); issue_kv(); }
    WAIT_BAR(0);
    if (w == 0 && lane == 0) set_flag(ws, FL_H + (r * 32 + c) * 8, epoch);
  }
  STAMP(4);
  const bool hasD = ii < 28;
  const int tt64 = xcd * 4 + (ii & 3), ct64 = ii >> 2;
  const int tt = w & 1, kq = w >> 1;
  const int tbd = tt64 * 2 + tt, n0 = ct64 * 64, rt = tt64 >> 2;
  const rsrc_t rs_d = mk(wd, F_ * D_ * 2), rs_o = mk(wo, D_ * D_ * 2);
  const int ch8d = k8 ^ sw;
  const unsigned wvo = (unsigned)(r8 * (D_ * 2) + n0 * 2 + ch8d * 16);
#define DPAGE(s) ((s) < 14 ? 48 + 8 * (s) : ((s) < 20 ? 8 * ((s) - 14) : 48 + 8 * ((s) - 20)))
  const int rotd = 4 * ((ct64 + 2 * (ii & 3)) % 7);
#define RD(s) (((s) + rotd >= 28) ? (s) + rotd - 28 : (s) + rotd)
#define ISSUE_D(s) bdma(lds0 + (DPAGE(s) + w) * 1024, wvo, rs_d, (unsigned)(448 * (w >> 1) + 16 * RD(s) + 8 * (w & 1)) * (D_ * 2))
  if (hasD && w < 4) {
#pragma unroll
    for (int s = 0; s < 14; ++s) ISSUE_D(s);
  }
  u32x4 hf[28];
  bool h_early = false;
  int hidx = -1;
  if (lane < 28) { const int fs = 28 * kq + lane; const int cc = fs < 64 ? fs >> 2 : 16 + (fs - 64) / 3; hidx = FL_H + ((tbd >> 3) * 32 + cc) * 8; }
  auto issue_h = [&]() {
#pragma unroll
    for (int s = 0; s < 28; ++s) hf[s] = LD128(rs_ws, l16, WS_HF + (unsigned)(tbd * 112 + kq * 28 + RD(s)) * 1024);
  };
  {
    const int mq = lane & 15, g = lane >> 4;
    const int t0 = w & ~1; const bool even = (w & 1) == 0;
    const float NEG = -__builtin_inff();
    f32x4 st[18];
    {
      f32x4 edge, diag;
#pragma unroll
      for (int e = 0; e < 4; ++e) { const int row = 4 * g + e; edge[e] = row > mq ? 0.f : NEG; diag[e] = row <= mq ? 0.f : NEG; }
      const f32x4 zero = {0.f, 0.f, 0.f, 0.f}, negv = {NEG, NEG, NEG, NEG};
      LAS char* kb = smem + t0 * 1024 + lane * 16;
      if (qb >= 2) {
#pragma unroll
        for (int T = 0; T < 18; ++T) {
          f32x4 cin = zero;
          if (T == 0) cin = even ? edge : negv;
          if (T == 1) cin = even ? zero : edge;
          if (T == 16) cin = even ? diag : zero;
          if (T == 17) cin = even ? negv : diag;
          st[T] = MFMA16(*(LAS bf16x8*)(kb + T * 1024), qf, cin);
        }
      } else {
#pragma unroll
        for (int T = 0; T < 18; ++T) {
          f32x4 cin = zero;
          if (T == 0) cin = even ? edge : negv;
          if (T == 1) cin = even ? zero : edge;
          if (T == 16) cin = even ? diag : zero;
          if (T == 17) cin = even ? negv : diag;
          if (kt0 + t0 + T < 0) cin = negv;
          st[T] = MFMA16(*(LAS bf16x8*)(kb + T * 1024), qf, cin);
        }
      }
    }
    const float SC = 0.18898223650461362f, L2E = 1.4426950408889634f;
    float mx = NEG;
#pragma unroll
    for (int T = 0; T < 18; ++T)
#pragma unroll
      for (int e = 0; e < 4; ++e) mx = fmaxf(mx, st[T][e]);
    { auto rr = __builtin_amdgcn_permlane16_swap(__float_as_uint(mx), __float_as_uint(mx), false, false); mx = fmaxf(__uint_as_float(rr[0]), __uint_as_float(rr[1])); }
    { auto rr = __builtin_amdgcn_permlane32_swap(__float_as_uint(mx), __float_as_uint(mx), false, false); mx = fmaxf(__uint_as_float(rr[0]), __uint_as_float(rr[1])); }
    const float C2 = SC * L2E;
    const float mc = -bfr(mx) * C2;
    if (hasD && w >= 4) {
#pragma unroll
      for (int s = 0; s < 14; ++s) ISSUE_D(s);
    }
    STAMP(5);
    f32x4 o0 = {0.f, 0.f, 0.f, 0.f}, o1 = {0.f, 0.f, 0.f, 0.f};
    u64 fvh = 0;
#pragma unroll
    for (int a = 0; a < 9; ++a) {
      if (a == 2) fvh = flag_ld(ws, hidx);
      if (a == 5) {
        h_early = hasD && __all(hidx < 0 || fvh == epoch);
        __builtin_amdgcn_fence(__ATOMIC_ACQUIRE, "wavefront");
        if (h_early) issue_h();
      }
      u32x4 pp;
#pragma unroll
      for (int hh = 0; hh < 2; ++hh)
#pragma unroll
        for (int e = 0; e < 2; ++e) {
          { unsigned r1 = pk2(st[2 * a + hh][2 * e], st[2 * a + hh][2 * e + 1]); PIN(r1);
          pp[2 * hh + e] = pk2(__builtin_amdgcn_exp2f(fmaf(bflo(r1), C2, mc)), __builtin_amdgcn_exp2f(fmaf(bfhi(r1), C2, mc))); }
        }
      LAS char* vb = smem + 24576 + ((t0 >> 1) + a) * 2048 + lane * 16;
      o0 = MFMA16(*(LAS bf16x8*)(vb), BC8(pp), o0);
      o1 = MFMA16(*(LAS bf16x8*)(vb + 1024), BC8(pp), o1);
    }
    const float l = __shfl(o1[0], 48 + mq);
    const float rl = 1.f / l;
    u32x4 ov;
    ov[0] = pk2(o0[0] * rl, o0[1] * rl); ov[1] = pk2(o0[2] * rl, o0[3] * rl);
    if (g == 3) { ov[2] = 0; ov[3] = 0; } else { ov[2] = pk2(o1[0] * rl, o1[1] * rl); ov[3] = pk2(o1[2] * rl, o1[3] * rl); }
    const int tok = w * 16 + mq;
    ST128(ov, rs_ws, (unsigned)((((tok >> 5) * 32 + (g >> 1)) * 64 + (tok & 31) + 32 * (g & 1)) * 16), WS_OF + (unsigned)((qb * 4 * 16 + head) * 2) * 1024);
  }
  STAMP(6);
  if (!hasD) {
    WAIT_VM(0);
    if (lane == 0) set_flag(ws, FL_O + (head * 16 + qb) * 8 + w, epoch);
    if (bid == 255 && w == 0) {
#pragma unroll 1
      for (int k = 0; k < 4; ++k) poll_flags(ws, FL_H + (k * 64 + lane) * 8, epoch);
      if (lane == 0) __hip_atomic_store((GAS u64*)(ws + WS_FL), epoch, __ATOMIC_RELAXED, __HIP_MEMORY_SCOPE_AGENT);
    }
    return;
  }
  {
    if (!h_early) { poll_flags(ws, hidx, epoch); issue_h(); }
    WAIT_BAR_L(29);
    STAMP(7);
#pragma unroll
    for (int s = 14; s < 20; ++s) ISSUE_D(s);
    const size_t rowoff = (size_t)(tbd * 32 + m) * D_ + n0 + (kq >> 1) * 32 + 16 * (kq & 1) + 4 * h;
    const u32x2 xv0 = *(const u32x2*)(x + rowoff), xv1 = *(const u32x2*)(x + rowoff + 8);
    f32x16 y0, y1, s0, s1;
#pragma unroll
    for (int i = 0; i < 16; ++i) { y0[i] = 0.f; y1[i] = 0.f; s0[i] = 0.f; s1[i] = 0.f; }
    const unsigned wlo = (unsigned)(kq * 2048 + (4 * h + q4) * 128 + blk * 32 + p4 * 8);
    LAS char* wl0 = smem + wlo + (((q4 >> 1) & 1) * 64);
    LAS char* wl1 = smem + wlo + ((1 ^ ((q4 >> 1) & 1)) * 64);
#define DF0(s) tr_frag(wl0, DPAGE(s) * 1024, DPAGE(s) * 1024 + 1024)
#define DF1(s) tr_frag(wl1, DPAGE(s) * 1024, DPAGE(s) * 1024 + 1024)
#define D_RANGE(S0, S1) do { bf16x8 fa[3], fb[3]; \
      _Pragma("unroll") for (int s = 0; s < 3; ++s) if ((S0) + s < (S1)) { fa[s] = DF0((S0) + s); fb[s] = DF1((S0) + s); } \
      _Pragma("unroll") for (int s = (S0); s < (S1); ++s) { \
        y0 = MFMA32(fa[(s - (S0)) % 3], BC8(hf[s]), y0); if (s + 3 < (S1)) fa[(s - (S0)) % 3] = DF0(s + 3); SBAR(); \
        y1 = MFMA32(fb[(s - (S0)) % 3], BC8(hf[s]), y1); if (s + 3 < (S1)) fb[(s - (S0)) % 3] = DF1(s + 3); SBAR(); } PIN(y0); PIN(y1); } while (0)
    D_RANGE(0, 8);
    WAIT_BAR(6);
    if (lane == 0) set_flag(ws, FL_O + (head * 16 + qb) * 8 + w, epoch);
#pragma unroll
    for (int s = 20; s < 28; ++s) ISSUE_D(s);
    const int oidx = lane < 8 ? FL_O + ((4 * kq + (lane >> 1)) * 16 + (tbd >> 2)) * 8 + 2 * (tbd & 3) + (lane & 1) : -1;
    D_RANGE(8, 14);
    WAIT_BAR(8);
    STAMP(8);
    {
#pragma unroll
      for (int j = 0; j < 6; ++j) {
        const int n = w * 6 + j, kk = n >> 4, i = n & 15;
        int row = 112 * kk + 8 * i + r8; if (row > 447) row = 447;
        bdma(lds0 + (112 + n) * 1024, (unsigned)(row * (D_ * 2) + n0 * 2 + ch8d * 16), rs_o, 0u);
      }
    }
    const u64 fvo = flag_ld(ws, oidx);
    D_RANGE(14, 16);
    BAR();
    {
#pragma unroll
      for (int j = 0; j < 2; ++j) {
        const int i = w * 2 + j;
        int row = 336 + 8 * i + r8; if (row > 447) row = 447;
        bdma(lds0 + i * 1024, (unsigned)(row * (D_ * 2) + n0 * 2 + ch8d * 16), rs_o, 0u);
      }
    }
    if (!__all(oidx < 0 || fvo == epoch)) poll_flags(ws, oidx, epoch);
    __builtin_amdgcn_fence(__ATOMIC_ACQUIRE, "wavefront");
    u32x4 of[8];
#pragma unroll
    for (int s = 0; s < 8; ++s) of[s] = LD128(rs_ws, l16, WS_OF + (unsigned)((tbd * 16 + kq * 4) * 2 + s) * 1024);
    D_RANGE(16, 20);
    WAIT_BAR(16);
    STAMP(9);
    D_RANGE(20, 28);
    WAIT_BAR(8);
    STAMP(10);
    {
      LAS char* base = smem + (kq < 3 ? (112 + 16 * kq) * 1024 : 0) + blk * 32 + p4 * 8;
#pragma unroll
      for (int hd = 0; hd < 4; ++hd)
#pragma unroll
        for (int s = 0; s < 2; ++s) {
          const int rb = 28 * hd + 8 * s;
          const int row0 = rb + 4 * h + q4, row1 = row0 + 16;
          const int o00 = (row0 >> 3) * 1024 + (row0 & 7) * 128, o01 = (row1 >> 3) * 1024 + (row1 & 7) * 128;
          const int x0 = ((row0 >> 1) & 1) * 64, x1 = ((row1 >> 1) & 1) * 64;
          const bf16x8 a0 = tr_frag(base, o00 + x0, o01 + x1);
          const bf16x8 a1 = tr_frag(base, o00 + (x0 ^ 64), o01 + (x1 ^ 64));
          s0 = MFMA32(a0, BC8(of[hd * 2 + s]), s0);
          s1 = MFMA32(a1, BC8(of[hd * 2 + s]), s1);
        }
    }
    PIN(s0); PIN(s1);
    LBAR();
    STAMP(11);
    {
      LAS f32x4* red = (LAS f32x4*)smem;
#pragma unroll
      for (int qd = 0; qd < 4; ++qd) {
        if (qd != kq) {
          const f32x16& yy = (qd >> 1) ? y1 : y0; const f32x16& ss = (qd >> 1) ? s1 : s0;
          const int b = 8 * (qd & 1);
          LAS f32x4* dst = red + (size_t)(((tt * 4 + kq) * 4 + qd) * 4) * 64 + lane;
          dst[0] = (f32x4){yy[b], yy[b + 1], yy[b + 2], yy[b + 3]}; dst[64] = (f32x4){yy[b + 4], yy[b + 5], yy[b + 6], yy[b + 7]};
          dst[128] = (f32x4){ss[b], ss[b + 1], ss[b + 2], ss[b + 3]}; dst[192] = (f32x4){ss[b + 4], ss[b + 5], ss[b + 6], ss[b + 7]};
        }
      }
      LBAR();
      f32x4 ya, yb, sa, sb;
      {
        const bool c1 = (kq >> 1) != 0, hh = (kq & 1) != 0;
#pragma unroll
        for (int e = 0; e < 4; ++e) {
          const float ylo = c1 ? y1[e] : y0[e], yhi = c1 ? y1[8 + e] : y0[8 + e], ylo2 = c1 ? y1[4 + e] : y0[4 + e], yhi2 = c1 ? y1[12 + e] : y0[12 + e];
          const float slo = c1 ? s1[e] : s0[e], shi = c1 ? s1[8 + e] : s0[8 + e], slo2 = c1 ? s1[4 + e] : s0[4 + e], shi2 = c1 ? s1[12 + e] : s0[12 + e];
          ya[e] = hh ? yhi : ylo; yb[e] = hh ? yhi2 : ylo2; sa[e] = hh ? shi : slo; sb[e] = hh ? shi2 : slo2;
        }
      }
#pragma unroll
      for (int k = 1; k < 4; ++k) {
        const int src = (kq + k) & 3;
        LAS f32x4* p = red + (size_t)(((tt * 4 + src) * 4 + kq) * 4) * 64 + lane;
        ya += p[0]; yb += p[64]; sa += p[128]; sb += p[192];
      }
#pragma unroll
      for (int i = 0; i < 2; ++i) {
        const f32x4 yv = i ? yb : ya, sv = i ? sb : sa;
        const u32x2 xv = i ? xv1 : xv0;
        float o[4];
#pragma unroll
        for (int e = 0; e < 4; ++e) {
          const float yy = yv[e];
          const float sg = yy * __builtin_amdgcn_rcpf(1.f + __builtin_amdgcn_exp2f(-yy * 1.4426950408889634f));
          const float xe = (e & 1) ? bfhi(xv[e >> 1]) : bflo(xv[e >> 1]);
          o[e] = xe + sv[e] * sg;
        }
        u32x2 ov = {pk2(o[0], o[1]), pk2(o[2], o[3])};
        *(u32x2*)(out + rowoff + 8 * i) = ov;
      }
    }
  }
  STAMP(12);
}

extern "C" void kernel_launch(void* const* d_in, const int* in_sizes, int n_in, void* d_out, int out_size, void* d_ws, size_t ws_size,
                              hipStream_t stream) {
  (void)hipFuncSetAttribute((const void*)mega, hipFuncAttributeMaxDynamicSharedMemorySize, 163840);
  mega<<<256, 512, 163840, stream>>>((const u16*)d_in[0], (const u16*)d_in[1], (const u16*)d_in[2], (const u16*)d_in[4], (const u16*)d_in[5],
                                     (const u16*)d_in[6], (u16*)d_out, (char*)d_ws);
}
```

```cpp
#include <hip/hip_runtime.h>
#include <hip/hip_bf16.h>
#include <stdint.h>

#define LAS __attribute__((address_space(3)))
#define GAS __attribute__((address_space(1)))
#define DI __device__ __forceinline__

typedef unsigned short u16;
typedef unsigned long long u64;
typedef __bf16 bf16x8 __attribute__((ext_vector_type(8)));
typedef __bf16 bf16x2 __attribute__((ext_vector_type(2)));
typedef short s16x4 __attribute__((ext_vector_type(4)));
typedef short s16x8 __attribute__((ext_vector_type(8)));
typedef float f32x4 __attribute__((ext_vector_type(4)));
typedef float f32x16 __attribute__((ext_vector_type(16)));
typedef unsigned u32x4 __attribute__((ext_vector_type(4)));
typedef unsigned u32x2 __attribute__((ext_vector_type(2)));
typedef __amdgpu_buffer_rsrc_t rsrc_t;

constexpr int S_ = 2048, D_ = 448, F_ = 1792, NQKV = 1344;
constexpr unsigned WS_QF = 0, WS_KF = 2u << 20, WS_VF = 4u << 20, WS_OF = 6u << 20, WS_HF = 8u << 20, WS_XF = 16u << 20, WS_FL = 18u << 20, WS_BYTES = 19u << 20;
constexpr unsigned FL_LN = 0, FL_Q = 512, FL_H = 2560, FL_O = 4608;
constexpr unsigned SPIN_CAP = 1u << 16;

#define XPF 10
#define KV0 6
#define KV1 11
#define KV2 13
#define KV3 18
#define SBAR() __builtin_amdgcn_sched_barrier(0)
#define PIN(x) asm volatile("" : "+v"(x))
#define WAIT_VM(N) asm volatile("s_waitcnt vmcnt(" #N ")" ::: "memory")
#define BAR() asm volatile("s_barrier" ::: "memory")
#define WAIT_BAR(N) asm volatile("s_waitcnt vmcnt(" #N ")\n\ts_barrier" ::: "memory")
#define WAIT_BAR_L(N) asm volatile("s_waitcnt vmcnt(" #N ") lgkmcnt(0)\n\ts_barrier" ::: "memory")
#define LBAR() asm volatile("s_waitcnt lgkmcnt(0)\n\ts_barrier" ::: "memory")

DI rsrc_t mk(const void* p, unsigned bytes) { return __builtin_amdgcn_make_buffer_rsrc((void*)p, (short)0, (int)bytes, 0x00020000); }
DI void bdma(unsigned ldsaddr, unsigned voff, rsrc_t rs, unsigned soff) {
  asm volatile("s_mov_b32 m0, %0\n\ts_nop 0\n\tbuffer_load_dwordx4 %1, %2, %3 offen lds" :: "s"(ldsaddr), "v"(voff), "s"(rs), "s"(soff) : "memory");
}
DI void bdma_sc1(unsigned ldsaddr, unsigned voff, rsrc_t rs, unsigned soff) {
  asm volatile("s_mov_b32 m0, %0\n\ts_nop 0\n\tbuffer_load_dwordx4 %1, %2, %3 offen sc1 lds" :: "s"(ldsaddr), "v"(voff), "s"(rs), "s"(soff) : "memory");
}
DI bf16x8 tr_frag(LAS char* p, int off0, int off1) {
  s16x4 lo = __builtin_amdgcn_ds_read_tr16_b64_v4i16((LAS s16x4*)(p + off0));
  s16x4 hi = __builtin_amdgcn_ds_read_tr16_b64_v4i16((LAS s16x4*)(p + off1));
  s16x8 v = __builtin_shufflevector(lo, hi, 0, 1, 2, 3, 4, 5, 6, 7);
  return __builtin_bit_cast(bf16x8, v);
}
DI unsigned pk2(float a, float b) { bf16x2 c; c[0] = (__bf16)a; c[1] = (__bf16)b; return __builtin_bit_cast(unsigned, c); }
DI float bflo(unsigned u) { return __uint_as_float(u << 16); }
DI float bfhi(unsigned u) { return __uint_as_float(u & 0xffff0000u); }
DI float bfr(float a) { return bflo(pk2(a, 0.f)); }
#define MFMA32(a, b, c) __builtin_amdgcn_mfma_f32_32x32x16_bf16((a), (b), (c), 0, 0, 0)
#define MFMA16(a, b, c) __builtin_amdgcn_mfma_f32_16x16x32_bf16((a), (b), (c), 0, 0, 0)
#define ST128(v, rs, voff, soff) __builtin_amdgcn_raw_buffer_store_b128((v), (rs), (voff) + (soff), 0, 16)
#define LD128(rs, voff, soff) __builtin_amdgcn_raw_buffer_load_b128((rs), (voff), (soff), 16)
#define BC8(v) __builtin_bit_cast(bf16x8, v)

template <int CTRL> DI float dpp_add(float v) {
  return v + __builtin_bit_cast(float, __builtin_amdgcn_update_dpp(0, __builtin_bit_cast(int, v), CTRL, 0xf, 0xf, true));
}
DI float wave_sum(float v) {
  v = dpp_add<0x111>(v); v = dpp_add<0x112>(v); v = dpp_add<0x114>(v); v = dpp_add<0x118>(v);
  const float r0 = __builtin_bit_cast(float, __builtin_amdgcn_readlane(__builtin_bit_cast(int, v), 15));
  const float r1 = __builtin_bit_cast(float, __builtin_amdgcn_readlane(__builtin_bit_cast(int, v), 31));
  const float r2 = __builtin_bit_cast(float, __builtin_amdgcn_readlane(__builtin_bit_cast(int, v), 47));
  const float r3 = __builtin_bit_cast(float, __builtin_amdgcn_readlane(__builtin_bit_cast(int, v), 63));
  return (r0 + r1) + (r2 + r3);
}
DI float row_sum(float v) {
  v = dpp_add<0x124>(v); v = dpp_add<0x128>(v);
  { auto rr = __builtin_amdgcn_permlane16_swap(__float_as_uint(v), __float_as_uint(v), false, false); v = __uint_as_float(rr[0]) + __uint_as_float(rr[1]); }
  { auto rr = __builtin_amdgcn_permlane32_swap(__float_as_uint(v), __float_as_uint(v), false, false); v = __uint_as_float(rr[0]) + __uint_as_float(rr[1]); }
  return v;
}
DI GAS u64* flag_ptr(char* ws, int idx) { return (GAS u64*)(ws + WS_FL + 256 + (size_t)idx * 8); }
DI u64 flag_ld(char* ws, int idx) { return __hip_atomic_load(flag_ptr(ws, idx < 0 ? 0 : idx), __ATOMIC_RELAXED, __HIP_MEMORY_SCOPE_AGENT); }
DI void poll_flags(char* ws, int idx, u64 epoch) {
  for (unsigned spins = 0; spins < SPIN_CAP; ++spins) {
    const u64 v = flag_ld(ws, idx);
    if (__all(idx < 0 || v == epoch)) break;
    __builtin_amdgcn_s_sleep(3);
  }
  __builtin_amdgcn_fence(__ATOMIC_ACQUIRE, "wavefront");
}
DI void set_flag(char* ws, int idx, u64 epoch) { __hip_atomic_store(flag_ptr(ws, idx), epoch, __ATOMIC_RELAXED, __HIP_MEMORY_SCOPE_AGENT); }

__global__ __launch_bounds__(512, 2) void mega(const u16* __restrict__ x, const u16* __restrict__ wqkv, const u16* __restrict__ wo,
                                              const u16* __restrict__ wg, const u16* __restrict__ wu, const u16* __restrict__ wd,
                                              u16* __restrict__ out, char* __restrict__ ws) {
  extern __shared__ __attribute__((aligned(16))) char smem_[];
  LAS char* smem = (LAS char*)smem_;
  const unsigned lds0 = (unsigned)(uintptr_t)smem;
  const int tid = threadIdx.x, lane = tid & 63, w = __builtin_amdgcn_readfirstlane(tid >> 6);
  const int bid = blockIdx.x, xcd = bid & 7, ii = bid >> 3;
  const int m = lane & 31, h = lane >> 5;
  const int blk = (lane >> 4) & 1, q4 = (lane & 15) >> 2, p4 = lane & 3;
  const rsrc_t rs_ws = mk(ws, WS_BYTES);
  const unsigned l16 = (unsigned)lane * 16;

  const int r = (xcd & 1) * 4 + (ii & 3), c = (xcd >> 1) * 8 + (ii >> 2);
  const bool isP = c >= 16;
  const int pi = c - 16;
  const int tA = isP ? (pi < 8 ? 32 + 2 * pi : 2 * (pi - 8)) : 16 + c;
  const int rot = 7 * ((ii + (ii >> 2)) & 3);
  const bool isLN = xcd < 4;
  const int lne = ii * 4 + xcd;
  const int lpart = lane >> 2, lrow = lane & 3;
  u32x4 xr[4];
#pragma unroll
  for (int i = 0; i < 4; ++i) xr[i] = (u32x4){0, 0, 0, 0};
  if (isLN && w >= 4) {
    const u16* xp = x + (size_t)(lne * 16 + (w - 4) * 4 + lrow) * D_ + lpart * 8;
#pragma unroll
    for (int i = 0; i < 4; ++i) if (i < 3 || lpart < 8) xr[i] = *(const u32x4*)(xp + i * 128);
  }
  const u64 epoch = __hip_atomic_load((GAS u64*)(ws + WS_FL), __ATOMIC_RELAXED, __HIP_MEMORY_SCOPE_AGENT) + 1;
  const int r8 = lane >> 3, k8 = lane & 7, sw = ((r8 >> 1) & 1) << 2;
  const unsigned scr = isP ? 144u * 1024 : 28u * 1024;
  auto stream = [&](int kind) {
    const int hf = kind & 1;
    const bool isq = kind < 2;
    const u16* base = isq ? wqkv : (kind < 4 ? wg : wu);
    const rsrc_t rs = mk(base, isq ? D_ * NQKV * 2 : D_ * F_ * 2);
    const unsigned ld2 = isq ? NQKV * 2 : F_ * 2;
    unsigned vo, lds, lstep; bool act = true;
    if (!isP) {
      if (isq) { vo = (unsigned)((lane >> 2) * (NQKV * 2) + 56 * tA + (lane & 3) * 16); lds = 0; lstep = 1024; }
      else { vo = (unsigned)(r8 * (F_ * 2) + 128 * c + (k8 ^ sw ^ (kind < 4 ? 0 : 2)) * 16); lds = (unsigned)((kind < 4 ? 48 : 104) + hf) * 1024; lstep = 2048; }
    } else {
      if (isq) { int cB = 56 * (tA + 1); if (cB > 2624) cB = 2624; const int ch8 = k8 ^ sw;
                 vo = (unsigned)(r8 * (NQKV * 2) + (ch8 < 4 ? 56 * tA + 16 * ch8 : cB + 16 * (ch8 - 4))); lds = (unsigned)hf * 1024; lstep = 2048; }
      else { const int r6 = lane / 6, k6 = lane - 6 * r6; vo = (unsigned)(r6 * (F_ * 2) + 2 * (1024 + 48 * pi) + 16 * k6);
             lds = (kind < 4 ? 57344u : 100480u) + (unsigned)hf * 768; lstep = 1536; act = lane < 48; }
    }
    const unsigned so0 = (!isP && isq) ? 0u : (unsigned)hf * 8 * ld2, sstep = 16 * ld2, wrap = so0 + 28 * sstep;
    unsigned so = so0 + (unsigned)rot * sstep;
    lds += lds0;
    if (act) {
#pragma unroll 1
      for (int g = 0; g < 4; ++g) {
#pragma unroll
        for (int j = 0; j < 7; ++j) { bdma(lds, vo, rs, so); lds += lstep; so += sstep; }
        if (so >= wrap) so -= 28 * sstep;
      }
    }
  };
  const int ndw = isLN ? 4 : 6;
  if (w < ndw) {
    if (!isP) { if (w == 0) stream(0); stream(w < 2 ? 4 + w : w); }
    else stream(w);
  } else if (isLN) {
    __builtin_amdgcn_s_setprio(3);
#pragma unroll 1
    for (int pass = 1; pass < 2; ++pass) {
    u32x4 xc[4];
    if (pass) {
#pragma unroll
      for (int i = 0; i < 4; ++i) { PIN(xr[i]); xc[i] = xr[i]; }

    } else {
#pragma unroll
      for (int i = 0; i < 4; ++i) { xc[i] = (u32x4){1u, 2u, 3u, (unsigned)lane}; PIN(xc[i]); }
    }
    float f[32];
#pragma unroll
    for (int i = 0; i < 4; ++i)
#pragma unroll
      for (int e = 0; e < 4; ++e) { f[8 * i + 2 * e] = bflo(xc[i][e]); f[8 * i + 2 * e + 1] = bfhi(xc[i][e]); }
    float s0 = 0.f, s1 = 0.f, s2 = 0.f, s3 = 0.f;
#pragma unroll
    for (int e = 0; e < 8; ++e) { s0 += f[e]; s1 += f[8 + e]; s2 += f[16 + e]; s3 += f[24 + e]; }
    const float mean = row_sum((s0 + s1) + (s2 + s3)) * (1.f / 448.f);
    const bool tail = lpart < 8;
    s0 = 0.f; s1 = 0.f; s2 = 0.f; s3 = 0.f;
#pragma unroll
    for (int e = 0; e < 8; ++e) { const float d0 = f[e] - mean, d1 = f[8 + e] - mean, d2 = f[16 + e] - mean, d3 = f[24 + e] - mean;
                                  s0 = fmaf(d0, d0, s0); s1 = fmaf(d1, d1, s1); s2 = fmaf(d2, d2, s2); s3 = fmaf(d3, d3, s3); }
    const float var = row_sum((s0 + s1) + (s2 + (tail ? s3 : 0.f))) * (1.f / 448.f);
    const float mb = bfr(mean);
    const float den = bfr(sqrtf(bfr(bfr(var) + 1e-5f)));
    const float rd = 1.f / den;
    const unsigned xo = WS_XF + (unsigned)(lne >> 1) * 28672 + (unsigned)((lne & 1) * 16 + (w - 4) * 4 + lrow) * 16 + (unsigned)((lpart >> 1) * 1024 + (lpart & 1) * 512);
#pragma unroll
    for (int i = 0; i < 4; ++i) {
      float y[8];
#pragma unroll
      for (int e = 0; e < 8; ++e) { const float a = bfr(f[8 * i + e] - mb); const float q0 = a * rd; y[e] = fmaf(fmaf(-q0, den, a), rd, q0); }
      u32x4 o = {pk2(y[0], y[1]), pk2(y[2], y[3]), pk2(y[4], y[5]), pk2(y[6], y[7])}; PIN(o);
      if (pass && (i < 3 || tail)) ST128(o, rs_ws, xo, (unsigned)i * 8192);
    }
    }

    WAIT_VM(0);

    if (lane == 0) set_flag(ws, FL_LN + lne * 4 + (w - 4), epoch);
    __builtin_amdgcn_s_setprio(0);
  }
  const int tbg = r * 8 + w;
  if (w < ndw) { WAIT_VM(0);  }
  BAR();
  if (w == 7) {
    poll_flags(ws, FL_LN + r * 64 + lane, epoch);

  }
  BAR();
  u32x4 xf[28];
  const unsigned xvo = l16 + WS_XF + (unsigned)tbg * 28672;
#define XLOAD(j) xf[j] = LD128(rs_ws, xvo, (unsigned)((((j) + rot >= 28) ? (j) + rot - 28 : (j) + rot) * 1024))
#pragma unroll
  for (int j = 0; j < XPF; ++j) XLOAD(j);

  const int head = xcd + 8 * (ii >> 4), qb = ii & 15;
  int kvidx = -1;
  {
    int tb, cc;
    if (lane < 4) { tb = 4 * qb + lane; cc = 24 + (head >> 1); }
    else if (lane < 16) { tb = 4 * qb - 8 + (lane - 4); cc = head; }
    else { tb = 4 * qb - 8 + (lane - 16); cc = 16 + (head >> 1); }
    if (tb < 0) tb = 0;
    if (lane < 28) kvidx = FL_Q + ((tb >> 3) * 32 + cc) * 8 + (tb & 7);
  }
  const int kt0 = qb * 8 - 16;
  bf16x8 qf;
  auto issue_kv = [&]() {
#pragma unroll
    for (int j = 0; j < 6; ++j) {
      const int i = w * 6 + j;
      if (w < 4) { int kt = kt0 + i; if (kt < 0) kt = 0; bdma_sc1(lds0 + i * 1024, l16, rs_ws, WS_KF + (unsigned)(head * 128 + kt) * 1024); }
      else { const int jj = i - 24; int kb = (kt0 >> 1) + (jj >> 1); if (kb < 0) kb = 0;
             bdma_sc1(lds0 + i * 1024, l16, rs_ws, WS_VF + (unsigned)((head * 64 + kb) * 2 + (jj & 1)) * 1024); }
    }
    qf = BC8(LD128(rs_ws, l16, WS_QF + (unsigned)(head * 128 + qb * 8 + w) * 1024));
  };
  {
    auto epi_qk = [&](f32x16& acc, int t) {
      const int hd = t & 15;
      if (h == 1) { acc[12] = 0.f; acc[13] = 0.f; acc[14] = 0.f; acc[15] = 0.f; }
      u32x4 p0 = {pk2(acc[0], acc[1]), pk2(acc[2], acc[3]), pk2(acc[4], acc[5]), pk2(acc[6], acc[7])};
      u32x4 p1 = {pk2(acc[8], acc[9]), pk2(acc[10], acc[11]), pk2(acc[12], acc[13]), pk2(acc[14], acc[15])};
      const unsigned vo = (unsigned)(((m >> 4) * 64 + (m & 15) + 16 * h) * 16);
      const unsigned so = (t < 16 ? WS_QF : WS_KF) + (unsigned)(hd * 128 + tbg * 2) * 1024;
      ST128(p0, rs_ws, vo, so); ST128(p1, rs_ws, vo + 512, so);
    };
    auto epi_v = [&](f32x16& acc, int t) {
      const int hd = t - 32, shift = (t == 47) ? 4 : 0;
      const int d = (m - shift) & 31;
      if (d >= 28) {
#pragma unroll
        for (int i = 0; i < 16; ++i) acc[i] = (d == 28) ? 1.f : 0.f;
      }
      u32x4 p0 = {pk2(acc[0], acc[1]), pk2(acc[2], acc[3]), pk2(acc[8], acc[9]), pk2(acc[10], acc[11])};
      u32x4 p1 = {pk2(acc[4], acc[5]), pk2(acc[6], acc[7]), pk2(acc[12], acc[13]), pk2(acc[14], acc[15])};
      const unsigned vo = (unsigned)(((d >> 4) * 64 + (d & 15) + 16 * h) * 16);
      const unsigned so = WS_VF + (unsigned)(hd * 64 + tbg) * 2048;
      ST128(p0, rs_ws, vo, so); ST128(p1, rs_ws, vo + 512, so);
    };
    auto epi_h = [&](const f32x16& acc, int fs) {
      u32x4 p0;
#pragma unroll
      for (int e = 0; e < 4; ++e) p0[e] = pk2(acc[2 * e] * acc[8 + 2 * e], acc[2 * e + 1] * acc[9 + 2 * e]);
      ST128(p0, rs_ws, l16, WS_HF + (unsigned)(tbg * 112 + fs) * 1024);
    };
    f32x16 accA, accB, accC;
#pragma unroll
    for (int i = 0; i < 16; ++i) { accA[i] = 0.f; accB[i] = 0.f; accC[i] = 0.f; }
#define PASS3(PA, SA, TA, PB, SB, TB, PC, SC_, TC, SWAP) do { bf16x8 fa[2], fb[2], fc[2]; \
      _Pragma("unroll") for (int s = 0; s < 2; ++s) { fa[s] = tr_frag(PA, s * (SA), s * (SA) + (TA)); fb[s] = tr_frag(PB, s * (SB), s * (SB) + (TB)); fc[s] = tr_frag(PC, s * (SC_), s * (SC_) + (TC)); } \
      _Pragma("unroll") for (int s = 0; s < 28; ++s) { \
        accC = MFMA32(fc[s & 1], BC8(xf[s]), accC); \
        if (s + 2 < 28) fc[s & 1] = tr_frag(PC, (s + 2) * (SC_), (s + 2) * (SC_) + (TC)); \
        if (s + XPF < 28) XLOAD(s + XPF); \
        SBAR(); \
        if (SWAP) accA = MFMA32(BC8(xf[s]), fa[s & 1], accA); else accA = MFMA32(fa[s & 1], BC8(xf[s]), accA); \
        if (s + 2 < 28) fa[s & 1] = tr_frag(PA, (s + 2) * (SA), (s + 2) * (SA) + (TA)); \
        SBAR(); \
        if (SWAP) accB = MFMA32(BC8(xf[s]), fb[s & 1], accB); else accB = MFMA32(fb[s & 1], BC8(xf[s]), accB); \
        if (s + 2 < 28) fb[s & 1] = tr_frag(PB, (s + 2) * (SB), (s + 2) * (SB) + (TB)); \
        SBAR(); } } while (0)
#define PASS2(PA, PB, KS, TS, HOOK) do { bf16x8 fa[3], fb[3]; \
      _Pragma("unroll") for (int s = 0; s < 3; ++s) { fa[s] = tr_frag(PA, s * (KS), s * (KS) + (TS)); fb[s] = tr_frag(PB, s * (KS), s * (KS) + (TS)); } \
      _Pragma("unroll") for (int s = 0; s < 28; ++s) { \
        accA = MFMA32(fa[s % 3], BC8(xf[s]), accA); \
        if (s + 3 < 28) fa[s % 3] = tr_frag(PA, (s + 3) * (KS), (s + 3) * (KS) + (TS)); \
        SBAR(); \
        accB = MFMA32(fb[s % 3], BC8(xf[s]), accB); \
        if (s + 3 < 28) fb[s % 3] = tr_frag(PB, (s + 3) * (KS), (s + 3) * (KS) + (TS)); \
        SBAR(); \
        HOOK(s); } } while (0)
    bool kv_early = false;
    u64 fv = 0;
#define HOOK2(s) do { \
      if (s == KV0 || (s == KV2 && !kv_early)) fv = flag_ld(ws, kvidx); \
      if (s == KV1 || (s == KV3 && !kv_early)) { kv_early = __all(kvidx < 0 || fv == epoch); __builtin_amdgcn_fence(__ATOMIC_ACQUIRE, "wavefront"); if (kv_early) issue_kv(); } } while (0)
    if (!isP) {
      LAS char* ql = smem + h * 512 + q4 * 64 + blk * 32 + p4 * 8;
      const unsigned gu0 = (unsigned)((blk ? 104 : 48) * 1024 + h * 1024 + q4 * 128 + (((p4 >> 1) ^ (((q4 >> 1) & 1) << 2) ^ (blk << 1)) * 16) + (p4 & 1) * 8);
      LAS char* g0 = smem + gu0; LAS char* g1 = smem + (gu0 ^ 32u); LAS char* g2 = smem + (gu0 ^ 64u); LAS char* g3 = smem + (gu0 ^ 96u);
      PASS3(ql, 1024, 256, g0, 2048, 512, g1, 2048, 512, false);
      epi_qk(accA, tA); epi_h(accB, 4 * c); epi_h(accC, 4 * c + 1);

      WAIT_VM(0); if (lane == 0) set_flag(ws, FL_Q + (r * 32 + c) * 8 + w, epoch);
      BAR();
#pragma unroll
      for (int i = 0; i < 16; ++i) { accA[i] = 0.f; accB[i] = 0.f; }
      PASS2(g2, g3, 2048, 512, HOOK2);
      epi_h(accA, 4 * c + 2); epi_h(accB, 4 * c + 3);
    } else {
      LAS char* qa = smem + h * 1024 + q4 * 128 + ((((q4 >> 1) & 1)) * 64) + blk * 32 + p4 * 8;
      LAS char* qb_ = smem + h * 1024 + q4 * 128 + ((1 ^ ((q4 >> 1) & 1)) * 64) + blk * 32 + p4 * 8;
      const unsigned gu0 = (unsigned)((blk ? 100480 : 57344) + h * 768 + q4 * 96 + p4 * 8);
      LAS char* g0 = smem + gu0; LAS char* g1 = smem + gu0 + 32; LAS char* g2 = smem + gu0 + 64;
      if (pi < 8) {
        PASS3(qa, 2048, 512, qb_, 2048, 512, g0, 1536, 384, true);
        epi_v(accA, tA); epi_v(accB, tA + 1);
      } else {
        PASS3(qa, 2048, 512, qb_, 2048, 512, g0, 1536, 384, false);
        epi_qk(accA, tA); epi_qk(accB, tA + 1);
      }
      epi_h(accC, 64 + 3 * pi);

      WAIT_VM(0); if (lane == 0) set_flag(ws, FL_Q + (r * 32 + c) * 8 + w, epoch);
      BAR();
#pragma unroll
      for (int i = 0; i < 16; ++i) { accA[i] = 0.f; accB[i] = 0.f; }
      PASS2(g1, g2, 1536, 384, HOOK2);
      epi_h(accA, 64 + 3 * pi + 1); epi_h(accB, 64 + 3 * pi + 2);
    }

    if (!kv_early) { poll_flags(ws, kvidx, epoch); issue_kv(); }
    WAIT_BAR(0);
    if (w == 0 && lane == 0) set_flag(ws, FL_H + (r * 32 + c) * 8, epoch);
  }

  const bool hasD = ii < 28;
  const int tt64 = xcd * 4 + (ii & 3), ct64 = ii >> 2;
  const int tt = w & 1, kq = w >> 1;
  const int tbd = tt64 * 2 + tt, n0 = ct64 * 64, rt = tt64 >> 2;
  const rsrc_t rs_d = mk(wd, F_ * D_ * 2), rs_o = mk(wo, D_ * D_ * 2);
  const int ch8d = k8 ^ sw;
  const unsigned wvo = (unsigned)(r8 * (D_ * 2) + n0 * 2 + ch8d * 16);
#define DPAGE(s) ((s) < 14 ? 48 + 8 * (s) : ((s) < 20 ? 8 * ((s) - 14) : 48 + 8 * ((s) - 20)))
  const int rotd = 4 * ((ct64 + 2 * (ii & 3)) % 7);
#define RD(s) (((s) + rotd >= 28) ? (s) + rotd - 28 : (s) + rotd)
#define ISSUE_D(s) bdma(lds0 + (DPAGE(s) + w) * 1024, wvo, rs_d, (unsigned)(448 * (w >> 1) + 16 * RD(s) + 8 * (w & 1)) * (D_ * 2))
  if (hasD && w < 4) {
#pragma unroll
    for (int s = 0; s < 14; ++s) ISSUE_D(s);
  }
  u32x4 hf[28];
  bool h_early = false;
  int hidx = -1;
  if (lane < 28) { const int fs = 28 * kq + lane; const int cc = fs < 64 ? fs >> 2 : 16 + (fs - 64) / 3; hidx = FL_H + ((tbd >> 3) * 32 + cc) * 8; }
  auto issue_h = [&]() {
#pragma unroll
    for (int s = 0; s < 28; ++s) hf[s] = LD128(rs_ws, l16, WS_HF + (unsigned)(tbd * 112 + kq * 28 + RD(s)) * 1024);
  };
  {
    const int mq = lane & 15, g = lane >> 4;
    const int t0 = w & ~1; const bool even = (w & 1) == 0;
    const float NEG = -__builtin_inff();
    f32x4 st[18];
    {
      f32x4 edge, diag;
#pragma unroll
      for (int e = 0; e < 4; ++e) { const int row = 4 * g + e; edge[e] = row > mq ? 0.f : NEG; diag[e] = row <= mq ? 0.f : NEG; }
      const f32x4 zero = {0.f, 0.f, 0.f, 0.f}, negv = {NEG, NEG, NEG, NEG};
      LAS char* kb = smem + t0 * 1024 + lane * 16;
      if (qb >= 2) {
#pragma unroll
        for (int T = 0; T < 18; ++T) {
          f32x4 cin = zero;
          if (T == 0) cin = even ? edge : negv;
          if (T == 1) cin = even ? zero : edge;
          if (T == 16) cin = even ? diag : zero;
          if (T == 17) cin = even ? negv : diag;
          st[T] = MFMA16(*(LAS bf16x8*)(kb + T * 1024), qf, cin);
        }
      } else {
#pragma unroll
        for (int T = 0; T < 18; ++T) {
          f32x4 cin = zero;
          if (T == 0) cin = even ? edge : negv;
          if (T == 1) cin = even ? zero : edge;
          if (T == 16) cin = even ? diag : zero;
          if (T == 17) cin = even ? negv : diag;
          if (kt0 + t0 + T < 0) cin = negv;
          st[T] = MFMA16(*(LAS bf16x8*)(kb + T * 1024), qf, cin);
        }
      }
    }
    const float SC = 0.18898223650461362f, L2E = 1.4426950408889634f;
    float mx = NEG;
#pragma unroll
    for (int T = 0; T < 18; ++T)
#pragma unroll
      for (int e = 0; e < 4; ++e) mx = fmaxf(mx, st[T][e]);
    { auto rr = __builtin_amdgcn_permlane16_swap(__float_as_uint(mx), __float_as_uint(mx), false, false); mx = fmaxf(__uint_as_float(rr[0]), __uint_as_float(rr[1])); }
    { auto rr = __builtin_amdgcn_permlane32_swap(__float_as_uint(mx), __float_as_uint(mx), false, false); mx = fmaxf(__uint_as_float(rr[0]), __uint_as_float(rr[1])); }
    const float C2 = SC * L2E;
    const float mc = -bfr(mx) * C2;
    if (hasD && w >= 4) {
#pragma unroll
      for (int s = 0; s < 14; ++s) ISSUE_D(s);
    }

    f32x4 o0 = {0.f, 0.f, 0.f, 0.f}, o1 = {0.f, 0.f, 0.f, 0.f};
    u64 fvh = 0;
#pragma unroll
    for (int a = 0; a < 9; ++a) {
      if (a == 2) fvh = flag_ld(ws, hidx);
      if (a == 5) {
        h_early = hasD && __all(hidx < 0 || fvh == epoch);
        __builtin_amdgcn_fence(__ATOMIC_ACQUIRE, "wavefront");
        if (h_early) issue_h();
      }
      u32x4 pp;
#pragma unroll
      for (int hh = 0; hh < 2; ++hh)
#pragma unroll
        for (int e = 0; e < 2; ++e) {
          { unsigned r1 = pk2(st[2 * a + hh][2 * e], st[2 * a + hh][2 * e + 1]); PIN(r1);
          pp[2 * hh + e] = pk2(__builtin_amdgcn_exp2f(fmaf(bflo(r1), C2, mc)), __builtin_amdgcn_exp2f(fmaf(bfhi(r1), C2, mc))); }
        }
      LAS char* vb = smem + 24576 + ((t0 >> 1) + a) * 2048 + lane * 16;
      o0 = MFMA16(*(LAS bf16x8*)(vb), BC8(pp), o0);
      o1 = MFMA16(*(LAS bf16x8*)(vb + 1024), BC8(pp), o1);
    }
    const float l = __shfl(o1[0], 48 + mq);
    const float rl = 1.f / l;
    u32x4 ov;
    ov[0] = pk2(o0[0] * rl, o0[1] * rl); ov[1] = pk2(o0[2] * rl, o0[3] * rl);
    if (g == 3) { ov[2] = 0; ov[3] = 0; } else { ov[2] = pk2(o1[0] * rl, o1[1] * rl); ov[3] = pk2(o1[2] * rl, o1[3] * rl); }
    const int tok = w * 16 + mq;
    ST128(ov, rs_ws, (unsigned)((((tok >> 5) * 32 + (g >> 1)) * 64 + (tok & 31) + 32 * (g & 1)) * 16), WS_OF + (unsigned)((qb * 4 * 16 + head) * 2) * 1024);
  }

  if (!hasD) {
    WAIT_VM(0);
    if (lane == 0) set_flag(ws, FL_O + (head * 16 + qb) * 8 + w, epoch);
    if (bid == 255 && w == 0) {
#pragma unroll 1
      for (int k = 0; k < 4; ++k) poll_flags(ws, FL_H + (k * 64 + lane) * 8, epoch);
      if (lane == 0) __hip_atomic_store((GAS u64*)(ws + WS_FL), epoch, __ATOMIC_RELAXED, __HIP_MEMORY_SCOPE_AGENT);
    }
    return;
  }
  {
    if (!h_early) { poll_flags(ws, hidx, epoch); issue_h(); }
    WAIT_BAR_L(29);

#pragma unroll
    for (int s = 14; s < 20; ++s) ISSUE_D(s);
    const size_t rowoff = (size_t)(tbd * 32 + m) * D_ + n0 + (kq >> 1) * 32 + 16 * (kq & 1) + 4 * h;
    const u32x2 xv0 = *(const u32x2*)(x + rowoff), xv1 = *(const u32x2*)(x + rowoff + 8);
    f32x16 y0, y1, s0, s1;
#pragma unroll
    for (int i = 0; i < 16; ++i) { y0[i] = 0.f; y1[i] = 0.f; s0[i] = 0.f; s1[i] = 0.f; }
    const unsigned wlo = (unsigned)(kq * 2048 + (4 * h + q4) * 128 + blk * 32 + p4 * 8);
    LAS char* wl0 = smem + wlo + (((q4 >> 1) & 1) * 64);
    LAS char* wl1 = smem + wlo + ((1 ^ ((q4 >> 1) & 1)) * 64);
#define DF0(s) tr_frag(wl0, DPAGE(s) * 1024, DPAGE(s) * 1024 + 1024)
#define DF1(s) tr_frag(wl1, DPAGE(s) * 1024, DPAGE(s) * 1024 + 1024)
#define D_RANGE(S0, S1) do { bf16x8 fa[3], fb[3]; \
      _Pragma("unroll") for (int s = 0; s < 3; ++s) if ((S0) + s < (S1)) { fa[s] = DF0((S0) + s); fb[s] = DF1((S0) + s); } \
      _Pragma("unroll") for (int s = (S0); s < (S1); ++s) { \
        y0 = MFMA32(fa[(s - (S0)) % 3], BC8(hf[s]), y0); if (s + 3 < (S1)) fa[(s - (S0)) % 3] = DF0(s + 3); SBAR(); \
        y1 = MFMA32(fb[(s - (S0)) % 3], BC8(hf[s]), y1); if (s + 3 < (S1)) fb[(s - (S0)) % 3] = DF1(s + 3); SBAR(); } PIN(y0); PIN(y1); } while (0)
    D_RANGE(0, 8);
    WAIT_BAR(6);
    if (lane == 0) set_flag(ws, FL_O + (head * 16 + qb) * 8 + w, epoch);
#pragma unroll
    for (int s = 20; s < 28; ++s) ISSUE_D(s);
    const int oidx = lane < 8 ? FL_O + ((4 * kq + (lane >> 1)) * 16 + (tbd >> 2)) * 8 + 2 * (tbd & 3) + (lane & 1) : -1;
    D_RANGE(8, 14);
    WAIT_BAR(8);

    {
#pragma unroll
      for (int j = 0; j < 6; ++j) {
        const int n = w * 6 + j, kk = n >> 4, i = n & 15;
        int row = 112 * kk + 8 * i + r8; if (row > 447) row = 447;
        bdma(lds0 + (112 + n) * 1024, (unsigned)(row * (D_ * 2) + n0 * 2 + ch8d * 16), rs_o, 0u);
      }
    }
    const u64 fvo = flag_ld(ws, oidx);
    D_RANGE(14, 16);
    BAR();
    {
#pragma unroll
      for (int j = 0; j < 2; ++j) {
        const int i = w * 2 + j;
        int row = 336 + 8 * i + r8; if (row > 447) row = 447;
        bdma(lds0 + i * 1024, (unsigned)(row * (D_ * 2) + n0 * 2 + ch8d * 16), rs_o, 0u);
      }
    }
    if (!__all(oidx < 0 || fvo == epoch)) poll_flags(ws, oidx, epoch);
    __builtin_amdgcn_fence(__ATOMIC_ACQUIRE, "wavefront");
    u32x4 of[8];
#pragma unroll
    for (int s = 0; s < 8; ++s) of[s] = LD128(rs_ws, l16, WS_OF + (unsigned)((tbd * 16 + kq * 4) * 2 + s) * 1024);
    D_RANGE(16, 20);
    WAIT_BAR(16);

    D_RANGE(20, 28);
    WAIT_BAR(8);

    f32x4 ya, yb, sa, sb;
#define XW(A0, A1, K, BASE, OLO, OHI) do { _Pragma("unroll") for (int qd = 0; qd < 4; ++qd) { const f32x16& v_ = (qd >> 1) ? A1 : A0; const int b_ = 8 * (qd & 1); \
      const f32x4 lo_ = {v_[b_], v_[b_ + 1], v_[b_ + 2], v_[b_ + 3]}, hi_ = {v_[b_ + 4], v_[b_ + 5], v_[b_ + 6], v_[b_ + 7]}; \
      if (qd == (K)) { OLO = lo_; OHI = hi_; } \
      else { const int j_ = (tt * 4 + (K)) * 3 + ((qd - (K) - 1) & 3); LAS f32x4* d_ = (LAS f32x4*)(smem + BASE(j_)) + lane; d_[0] = lo_; d_[64] = hi_; } } } while (0)
#define XW4(A0, A1, BASE, OLO, OHI) do { if (kq == 0) XW(A0, A1, 0, BASE, OLO, OHI); else if (kq == 1) XW(A0, A1, 1, BASE, OLO, OHI); else if (kq == 2) XW(A0, A1, 2, BASE, OLO, OHI); else XW(A0, A1, 3, BASE, OLO, OHI); } while (0)
#define YBASE(j) (49152 + 2048 * (j))
#define SBASE(j) (((j) < 16 ? 16384 : 65536) + 2048 * (j))
    XW4(y0, y1, YBASE, ya, yb);
    {
      LAS char* wb = smem + (kq < 3 ? (112 + 16 * kq) * 1024 : 0) + blk * 32 + p4 * 8;
      const int sx = ((q4 >> 1) & 1) * 64;
      LAS char* be0 = wb + (4 * h + q4) * 128 + sx;                       LAS char* be1 = wb + (4 * h + q4) * 128 + (sx ^ 64);
      LAS char* bo0 = wb + (h ? 1024 + q4 * 128 : (4 + q4) * 128) + sx;   LAS char* bo1 = wb + (h ? 1024 + q4 * 128 : (4 + q4) * 128) + (sx ^ 64);
#pragma unroll
      for (int hd = 0; hd < 4; ++hd)
#pragma unroll
        for (int s = 0; s < 2; ++s) {
          const int k0 = ((28 * hd + 8 * s) >> 3) * 1024;
          const bf16x8 a0 = tr_frag((hd & 1) ? bo0 : be0, k0, k0 + 2048);
          const bf16x8 a1 = tr_frag((hd & 1) ? bo1 : be1, k0, k0 + 2048);
          s0 = MFMA32(a0, BC8(of[hd * 2 + s]), s0);
          s1 = MFMA32(a1, BC8(of[hd * 2 + s]), s1);
        }
    }

    XW4(s0, s1, SBASE, sa, sb);
    LBAR();
    {
#pragma unroll
      for (int k = 1; k < 4; ++k) {
        const int src = (kq + k) & 3;
        const int j = (tt * 4 + src) * 3 + ((kq - src - 1) & 3);
        LAS f32x4* py = (LAS f32x4*)(smem + YBASE(j)) + lane;
        LAS f32x4* ps = (LAS f32x4*)(smem + SBASE(j)) + lane;
        ya += py[0]; yb += py[64]; sa += ps[0]; sb += ps[64];
      }
#pragma unroll
      for (int i = 0; i < 2; ++i) {
        const f32x4 yv = i ? yb : ya, sv = i ? sb : sa;
        const u32x2 xv = i ? xv1 : xv0;
        float o[4];
#pragma unroll
        for (int e = 0; e < 4; ++e) {
          const float yy = yv[e];
          const float sg = yy * __builtin_amdgcn_rcpf(1.f + __builtin_amdgcn_exp2f(-yy * 1.4426950408889634f));
          const float xe = (e & 1) ? bfhi(xv[e >> 1]) : bflo(xv[e >> 1]);
          o[e] = xe + sv[e] * sg;
        }
        u32x2 ov = {pk2(o[0], o[1]), pk2(o[2], o[3])};
        *(u32x2*)(out + rowoff + 8 * i) = ov;
      }
    }
  }

}

extern "C" void kernel_launch(void* const* d_in, const int* in_sizes, int n_in, void* d_out, int out_size, void* d_ws, size_t ws_size,
                              hipStream_t stream) {
  (void)hipFuncSetAttribute((const void*)mega, hipFuncAttributeMaxDynamicSharedMemorySize, 163840);
  mega<<<256, 512, 163840, stream>>>((const u16*)d_in[0], (const u16*)d_in[1], (const u16*)d_in[2], (const u16*)d_in[4], (const u16*)d_in[5],
                                     (const u16*)d_in[6], (u16*)d_out, (char*)d_ws);
}
```
